```python
import torch

def _softmax(x, axis=-1):
    return torch.softmax(x, dim=axis)

def _gelu(x):
    return torch.nn.functional.gelu(x, approximate='tanh')

def _silu(x):
    return torch.nn.functional.silu(x)

def _sigmoid(x):
    return torch.sigmoid(x)

def _layernorm(x, axis=-1, eps=1e-05):
    m = x.mean(dim=axis, keepdim=True)
    v = x.var(dim=axis, keepdim=True, unbiased=False)
    return (x - m) / torch.sqrt(v + eps)

def _scatter_add(tgt, idx, src, axis=0):
    return tgt.clone().index_add_(axis, idx, src)

def _segment_sum(src, seg, n):
    out = torch.zeros((n,) + src.shape[1:], dtype=src.dtype)
    return out.index_add_(0, seg, src)

def _pad(x, pw):
    flat = tuple((v for pair in reversed(pw) for v in pair))
    return torch.nn.functional.pad(x, flat)
kernel_name = 'adaLN_cond__sliding_block__dense_activation_squared_relu__prompt'

def setup_inputs():

    _w = lambda a: (lambda t: t.to(torch.bfloat16) if t.is_floating_point() else t)(torch.as_tensor(a))

    def _A_setup():
        B, S, D = (1, 2048, 512)
        return {'x': _w((lambda *s: torch.randn(*s, dtype=torch.bfloat16) * 0.05)(B, S, D)), 'wqkv': _w((lambda *s: torch.randn(*s, dtype=torch.bfloat16) * 0.05)(D, 3 * D)), 'wo': _w((lambda *s: torch.randn(*s, dtype=torch.bfloat16) * 0.05)(D, D))}
    _A_in = _A_setup()

    def _B_setup():
        B, S, D, F = (1, 2048, 512, 1792)
        return {'x': _w((lambda *s: torch.randn(*s, dtype=torch.bfloat16) * 0.05)(B, S, D)), 'w1': _w((lambda *s: torch.randn(*s, dtype=torch.bfloat16) * 0.05)(D, F)), 'b1': _w((lambda *s: torch.randn(*s, dtype=torch.bfloat16) * 0.4)(F)), 'w2': _w((lambda *s: torch.randn(*s, dtype=torch.bfloat16) * 0.05)(F, D)), 'b2': _w((lambda *s: torch.randn(*s, dtype=torch.bfloat16) * 3.2)(D))}
    _B_in = _B_setup()
    return {'A_x': _A_in['x'], 'A_wqkv': _A_in['wqkv'], 'A_wo': _A_in['wo'], 'B_x': _B_in['x'], 'B_w1': _B_in['w1'], 'B_b1': _B_in['b1'], 'B_w2': _B_in['w2'], 'B_b2': _B_in['b2'], 'c': _w((lambda *s: torch.randn(*s, dtype=torch.bfloat16) * 0.1)(1, 512) * 8.0), 'w_mod': _w((lambda *s: torch.randn(*s, dtype=torch.bfloat16) * 0.1)(512, 6 * 512) * 512 ** (-0.5) * 8.0)}

def reference(A_x, A_wqkv, A_wo, B_x, B_w1, B_b1, B_w2, B_b2, c, w_mod):

    def _A_ref(x, wqkv, wo):
        B, S, D = x.shape
        H = 32
        Dh = D // H
        W = 256
        qkv = (lambda x, *d: x.permute(*d))((x @ wqkv).reshape(B, S, 3, H, Dh), 2, 0, 3, 1, 4)
        q, k, v = (qkv[0], qkv[1], qkv[2])
        scores = torch.einsum('bhqd,bhkd->bhqk', q, k) * Dh ** (-0.5)
        idx = torch.arange(S)
        mask = (idx[:, None] - idx[None, :] >= 0) & (idx[:, None] - idx[None, :] < W)
        scores = torch.where(mask[None, None], scores, -1000000000.0)
        p = _softmax(scores, axis=-1)
        o = torch.einsum('bhqk,bhkd->bhqd', p, v)
        return (lambda x, *d: x.permute(*d))(o, 0, 2, 1, 3).reshape(B, S, D) @ wo

    def _B_ref(x, w1, b1, w2, b2):
        h = torch.einsum('bsd,df->bsf', x, w1) + b1
        zero = torch.zeros_like(h)
        r = torch.where(h > zero, h, zero)
        h = r * r
        out = torch.einsum('bsf,fd->bsd', h, w2) + b2
        return out
    x = A_x
    mod = (_silu(c) @ w_mod).reshape(c.shape[0], 6, 1, -1)
    sa, ca, ga, sb, cb, gb = (mod[:, 0].clone(), mod[:, 1].clone(), mod[:, 2].clone(), mod[:, 3].clone(), mod[:, 4].clone(), mod[:, 5].clone())
    h = _layernorm(x, axis=-1) * (1 + ca) + sa
    x = x + ga * _A_ref(h, A_wqkv, A_wo)
    h = _layernorm(x, axis=-1) * (1 + cb) + sb
    x = x + gb * _B_ref(h, B_w1, B_b1, B_w2, B_b2)
    return x
```

```cpp
#include <hip/hip_runtime.h>
#include <hip/hip_bf16.h>
#include <stdint.h>

typedef unsigned short u16;
typedef float f32x4 __attribute__((ext_vector_type(4)));
typedef short bf16x8 __attribute__((ext_vector_type(8)));

__device__ __forceinline__ float bf2f(u16 b) { return __builtin_bit_cast(float, (unsigned)b << 16); }
__device__ __forceinline__ u16 f2bf(float f) { __hip_bfloat16 h = __float2bfloat16(f); return __builtin_bit_cast(u16, h); }

constexpr int S = 2048, D = 512, F = 1792, H = 32, DH = 16, W = 256;

__global__ void k_mod(const u16* c, const u16* wmod, float* mod) {
  __shared__ float sc[512];
  __shared__ float red[4][64];
  int t = threadIdx.x;
  for (int i = t; i < 512; i += 256) { float v = bf2f(c[i]); float s = v / (1.f + __expf(-v)); sc[i] = bf2f(f2bf(s)); }
  __syncthreads();
  int col = blockIdx.x * 64 + (t & 63), g = t >> 6;
  float a = 0;
  for (int k = g * 128; k < g * 128 + 128; ++k) a += sc[k] * bf2f(wmod[k * 3072 + col]);
  red[g][t & 63] = a;
  __syncthreads();
  if (t < 64) mod[col] = bf2f(f2bf(red[0][t] + red[1][t] + red[2][t] + red[3][t]));
}

__global__ void k_ln(const u16* x, const float* shift, const float* scale, u16* h) {
  int row = blockIdx.x * 4 + (threadIdx.x >> 6), l = threadIdx.x & 63;
  float v[8]; float s = 0;
  for (int j = 0; j < 8; ++j) { v[j] = bf2f(x[row * D + l * 8 + j]); s += v[j]; }
  for (int o = 32; o; o >>= 1) s += __shfl_xor(s, o);
  float m = s / D; float q = 0;
  for (int j = 0; j < 8; ++j) { float d = v[j] - m; q += d * d; }
  for (int o = 32; o; o >>= 1) q += __shfl_xor(q, o);
  float r = rsqrtf(q / D + 1e-5f);
  for (int j = 0; j < 8; ++j) { int k = l * 8 + j; h[row * D + k] = f2bf((v[j] - m) * r * (1.f + scale[k]) + shift[k]); }
}

template <int MODE>
__global__ void k_gemm(const u16* A, const u16* B, int N, int K, u16* C, const u16* bias, const float* gate, const u16* res) {
  __shared__ u16 As[64][40];
  __shared__ u16 Bs[64][40];
  int t = threadIdx.x, w = t >> 6, l = t & 63;
  int m0 = blockIdx.y * 64, n0 = blockIdx.x * 64;
  int wm = (w >> 1) * 32, wn = (w & 1) * 32;
  f32x4 acc[2][2] = {};
  for (int k0 = 0; k0 < K; k0 += 32) {
    { int r = t >> 2, cc = (t & 3) * 8; *(uint4*)&As[r][cc] = *(const uint4*)&A[(size_t)(m0 + r) * K + k0 + cc]; }
    { int kk = t >> 3, nn = (t & 7) * 8; uint4 v = *(const uint4*)&B[(size_t)(k0 + kk) * N + n0 + nn]; const u16* p = (const u16*)&v; for (int j = 0; j < 8; ++j) Bs[nn + j][kk] = p[j]; }
    __syncthreads();
    bf16x8 a[2], b[2];
    for (int i = 0; i < 2; ++i) { a[i] = *(bf16x8*)&As[wm + i * 16 + (l & 15)][(l >> 4) * 8]; b[i] = *(bf16x8*)&Bs[wn + i * 16 + (l & 15)][(l >> 4) * 8]; }
    for (int i = 0; i < 2; ++i) for (int j = 0; j < 2; ++j) acc[i][j] = __builtin_amdgcn_mfma_f32_16x16x32_bf16(a[i], b[j], acc[i][j], 0, 0, 0);
    __syncthreads();
  }
  for (int i = 0; i < 2; ++i) for (int j = 0; j < 2; ++j) for (int r = 0; r < 4; ++r) {
    int m = m0 + wm + i * 16 + (l >> 4) * 4 + r, n = n0 + wn + j * 16 + (l & 15);
    float v = acc[i][j][r];
    if (MODE == 0) C[(size_t)m * N + n] = f2bf(v);
    if (MODE == 1) C[(size_t)m * N + n] = f2bf(bf2f(res[(size_t)m * N + n]) + gate[n] * v);
    if (MODE == 2) { v += bf2f(bias[n]); v = v > 0 ? v : 0; C[(size_t)m * N + n] = f2bf(v * v); }
    if (MODE == 3) C[(size_t)m * N + n] = f2bf(bf2f(res[(size_t)m * N + n]) + gate[n] * (v + bf2f(bias[n])));
  }
}

__global__ void k_attn(const u16* qkv, u16* o) {
  int q = blockIdx.x * 64 + (threadIdx.x & 63), h = blockIdx.y * 4 + (threadIdx.x >> 6);
  float qv[16], acc[16];
  for (int d = 0; d < 16; ++d) { qv[d] = bf2f(qkv[q * 1536 + h * 16 + d]) * 0.25f; acc[d] = 0; }
  float m = -1e30f, s = 0;
  int k0 = q - 255 < 0 ? 0 : q - 255;
  for (int k = k0; k <= q; ++k) {
    float sc = 0;
    for (int d = 0; d < 16; ++d) sc += qv[d] * bf2f(qkv[k * 1536 + 512 + h * 16 + d]);
    float mn = fmaxf(m, sc), al = __expf(m - mn), p = __expf(sc - mn);
    s = s * al + p;
    for (int d = 0; d < 16; ++d) acc[d] = acc[d] * al + p * bf2f(qkv[k * 1536 + 1024 + h * 16 + d]);
    m = mn;
  }
  for (int d = 0; d < 16; ++d) o[q * 512 + h * 16 + d] = f2bf(acc[d] / s);
}

extern "C" void kernel_launch(void* const* d_in, const int* in_sizes, int n_in, void* d_out, int out_size, void* d_ws, size_t ws_size, hipStream_t stream) {
  const u16* x = (const u16*)d_in[0]; const u16* wqkv = (const u16*)d_in[1]; const u16* wo = (const u16*)d_in[2];
  const u16* w1 = (const u16*)d_in[4]; const u16* b1 = (const u16*)d_in[5]; const u16* w2 = (const u16*)d_in[6]; const u16* b2 = (const u16*)d_in[7];
  const u16* c = (const u16*)d_in[8]; const u16* wmod = (const u16*)d_in[9];
  char* ws = (char*)d_ws;
  float* mod = (float*)ws; u16* h = (u16*)(ws + (1 << 20)); u16* qkv = (u16*)(ws + (4 << 20)); u16* o = (u16*)(ws + (12 << 20));
  u16* x1 = (u16*)(ws + (16 << 20)); u16* hf = (u16*)(ws + (20 << 20));
  k_mod<<<48, 256, 0, stream>>>(c, wmod, mod);
  k_ln<<<512, 256, 0, stream>>>(x, mod, mod + 512, h);
  k_gemm<0><<<dim3(24, 32), 256, 0, stream>>>(h, wqkv, 1536, 512, qkv, nullptr, nullptr, nullptr);
  k_attn<<<dim3(32, 8), 256, 0, stream>>>(qkv, o);
  k_gemm<1><<<dim3(8, 32), 256, 0, stream>>>(o, wo, 512, 512, x1, nullptr, mod + 1024, x);
  k_ln<<<512, 256, 0, stream>>>(x1, mod + 1536, mod + 2048, h);
  k_gemm<2><<<dim3(28, 32), 256, 0, stream>>>(h, w1, 1792, 512, hf, b1, nullptr, nullptr);
  k_gemm<3><<<dim3(8, 32), 256, 0, stream>>>(hf, w2, 512, 1792, (u16*)d_out, b2, mod + 2560, x1);
}
```

```cpp
#include <hip/hip_runtime.h>
#include <hip/hip_bf16.h>
#include <stdint.h>

#define PROF 0
#define MAPB 1

namespace {
typedef unsigned short u16;
typedef unsigned u32;
typedef unsigned long long u64;
typedef float f32x2 __attribute__((ext_vector_type(2)));
typedef float f32x4 __attribute__((ext_vector_type(4)));
typedef float f32x16 __attribute__((ext_vector_type(16)));
typedef u32 u32x2 __attribute__((ext_vector_type(2)));
typedef u32 u32x4 __attribute__((ext_vector_type(4)));
typedef short bf16x8 __attribute__((ext_vector_type(8)));
typedef short s16x4 __attribute__((ext_vector_type(4)));
typedef __bf16 bf16x2_t __attribute__((ext_vector_type(2)));
#define GAS __attribute__((address_space(1)))
#define LAS __attribute__((address_space(3)))
#define RLX_AGENT __ATOMIC_RELAXED, __HIP_MEMORY_SCOPE_AGENT
#define RLX_WG __ATOMIC_RELAXED, __HIP_MEMORY_SCOPE_WORKGROUP

constexpr u32 WS_EPOCH = 0, WS_MODG = 4096, WS_FLAG = 32768;
constexpr u32 WS_K = 1u << 20, WS_V = 3u << 20, WS_O = 5u << 20, WS_X = 7u << 20, WS_ST = 9u << 20, WS_H = 10u << 20, WS_PROF = 18u << 20, WS_END = 20u << 20;
enum { F_K = 0, F_V = 1, F_OA = 2, F_OB = 3, F_XA = 4, F_XB = 5, F_HA = 6, F_HB = 7 };
constexpr u32 L_KIMG = 0, L_VIMG = 40960, L_STG = 81920, L_Q = 147456, L_MOD = 155648, L_MISC = 161792;
constexpr u32 L_PART = L_MISC, L_STAT = L_MISC + 1024, L_DUMMY = L_MISC + 1536, L_ONES = L_MISC + 1792, L_CNT = L_MISC + 1808;
constexpr u32 L_WO = 0, L_XCH = 65536, L_X1 = 0;
constexpr u32 LDS_BYTES = 163840;
constexpr int SPIN_CAP = 1 << 18;
constexpr float QSCALE = 0.25f * 1.4426950408889634f;

__device__ __forceinline__ u32 pk(float a, float b) { f32x2 v = {a, b}; return __builtin_bit_cast(u32, __builtin_convertvector(v, bf16x2_t)); }
__device__ __forceinline__ float lo16(u32 v) { return __builtin_bit_cast(float, v << 16); }
__device__ __forceinline__ float hi16(u32 v) { return __builtin_bit_cast(float, v & 0xffff0000u); }
__device__ __forceinline__ float asf(u32 v) { return __builtin_bit_cast(float, v); }
__device__ __forceinline__ u32 asu(float v) { return __builtin_bit_cast(u32, v); }
template <int CTRL> __device__ __forceinline__ float dpp(float v) { return asf((u32)__builtin_amdgcn_update_dpp(0, (int)asu(v), CTRL, 0xF, 0xF, true)); }
__device__ __forceinline__ float add16(float a, float b) { auto r = __builtin_amdgcn_permlane16_swap(asu(a), asu(b), false, false); return asf(r[0]) + asf(r[1]); }
__device__ __forceinline__ float add32(float a, float b) { auto r = __builtin_amdgcn_permlane32_swap(asu(a), asu(b), false, false); return asf(r[0]) + asf(r[1]); }
constexpr int DPP_X1 = 0xB1, DPP_X2 = 0x4E, DPP_HMIR = 0x141, DPP_MIR = 0x140, DPP_ROR4 = 0x124, DPP_ROR8 = 0x128;

__device__ __forceinline__ u32x4 mkrs(const void* p, u32 bytes) { return u32x4{(u32)(uintptr_t)p, (u32)((uintptr_t)p >> 32), bytes, 0x00020000u}; }
#define DMA_L(o) "buffer_load_dwordx4 %2, %3, %0 offen offset:" #o " lds\n\t"
#define DMA_A "s_add_u32 %0, %0, %4\n\t"
#define DMA_G DMA_L(0) DMA_A DMA_L(1024) DMA_A DMA_L(2048) DMA_A DMA_L(3072)
#define DMA_N "s_add_u32 m0, m0, 0x1000\n\ts_add_u32 %0, %0, %5\n\t"
template <int STRIDE> __device__ __forceinline__ void dma4(u32 lds, u32 voff, u32x4 rs, u32 soff) {
  asm volatile("s_mov_b32 m0, %1\n\ts_nop 0\n\t" DMA_G : "+&s"(soff) : "s"(lds), "v"(voff), "s"(rs), "s"(STRIDE - 1024), "i"(STRIDE + 3072) : "memory", "scc");
}
template <int STRIDE> __device__ __forceinline__ void dma8(u32 lds, u32 voff, u32x4 rs, u32 soff) {
  asm volatile("s_mov_b32 m0, %1\n\ts_nop 0\n\t" DMA_G DMA_N DMA_G : "+&s"(soff) : "s"(lds), "v"(voff), "s"(rs), "s"(STRIDE - 1024), "i"(STRIDE + 3072) : "memory", "scc");
}
template <int STRIDE> __device__ __forceinline__ void dma16(u32 lds, u32 voff, u32x4 rs, u32 soff) {
  asm volatile("s_mov_b32 m0, %1\n\ts_nop 0\n\t" DMA_G DMA_N DMA_G DMA_N DMA_G DMA_N DMA_G : "+&s"(soff) : "s"(lds), "v"(voff), "s"(rs), "s"(STRIDE - 1024), "i"(STRIDE + 3072) : "memory", "scc");
}
__device__ __forceinline__ void dma4_sc1(u32 lds, u32 voff, u32x4 rs, u32 soff) {
  asm volatile("s_mov_b32 m0, %0\n\ts_nop 0\n\t"
               "buffer_load_dwordx4 %1, %2, %3 offen sc1 lds\n\t"
               "buffer_load_dwordx4 %1, %2, %3 offen offset:1024 sc1 lds\n\t"
               "buffer_load_dwordx4 %1, %2, %3 offen offset:2048 sc1 lds\n\t"
               "buffer_load_dwordx4 %1, %2, %3 offen offset:3072 sc1 lds" ::"s"(lds), "v"(voff), "s"(rs), "s"(soff) : "memory");
}
__device__ __forceinline__ void dma2_sc1(u32 lds, u32 voff, u32x4 rs, u32 soff) {
  asm volatile("s_mov_b32 m0, %0\n\ts_nop 0\n\t"
               "buffer_load_dwordx4 %1, %2, %3 offen sc1 lds\n\t"
               "buffer_load_dwordx4 %1, %2, %3 offen offset:1024 sc1 lds" ::"s"(lds), "v"(voff), "s"(rs), "s"(soff) : "memory");
}
__device__ __forceinline__ void touch(u32 voff, u32x4 rs, u32 soff) {
  asm volatile("s_mov_b32 m0, %0\n\ts_nop 0\n\tbuffer_load_dword %1, %2, %3 offen lds" ::"s"(L_DUMMY), "v"(voff), "s"(rs), "s"(soff) : "memory");
}
#define VMCNT(n) asm volatile("s_waitcnt vmcnt(" #n ")" ::: "memory")
#define BAR() asm volatile("s_waitcnt lgkmcnt(0)\n\ts_barrier" ::: "memory")
#define SCHED() __builtin_amdgcn_sched_barrier(0)
#define MFMA32(a, b, c) __builtin_amdgcn_mfma_f32_32x32x16_bf16(__builtin_bit_cast(bf16x8, a), __builtin_bit_cast(bf16x8, b), c, 0, 0, 0)
#define MFMA16(a, b, c) __builtin_amdgcn_mfma_f32_16x16x32_bf16(__builtin_bit_cast(bf16x8, a), __builtin_bit_cast(bf16x8, b), c, 0, 0, 0)

typedef LAS char* lptr;
__device__ __forceinline__ u32x4 lds128(lptr p) { return *(LAS u32x4*)p; }
__device__ __forceinline__ u32x2 ldstr(lptr p) { return __builtin_bit_cast(u32x2, __builtin_amdgcn_ds_read_tr16_b64_v4i16((LAS s16x4*)p)); }
__device__ __forceinline__ u32x4 trfrag(lptr p, int o0, int o1) { u32x2 a = ldstr(p + o0), b = ldstr(p + o1); return u32x4{a.x, a.y, b.x, b.y}; }

__device__ __forceinline__ void poll(const char* ws, u32 off, u64 tag) {
  const GAS u64* p = (const GAS u64*)(ws + off);
  for (int s = 0; s < SPIN_CAP; ++s) {
    const u64 f = __hip_atomic_load(p, RLX_AGENT);
    if (__all(f == tag)) break;
  }
  __builtin_amdgcn_fence(__ATOMIC_ACQUIRE, "wavefront");
  asm volatile("" ::: "memory");
}
__device__ __forceinline__ void set_flag(char* ws, int ph, int id, u64 tag) { __hip_atomic_store((GAS u64*)(ws + WS_FLAG + (ph * 256 + id) * 128), tag, RLX_AGENT); }
__device__ __forceinline__ u32 flag_off(int ph, int id) { return WS_FLAG + (ph * 256 + id) * 128; }
__device__ __forceinline__ int kvslot(int rb, int j) {
  return ((rb >> 3) * 2 + (j >> 2)) * 32 + (rb & 7) * 4 + (j & 3);
}

__device__ __forceinline__ void mod_partial(lptr L, u32x2 cv, const u32x4 (&wm)[4], int w, int lane) {
  float a[8];
#pragma unroll
  for (int i = 0; i < 4; ++i) {
    const u32 cw = i < 2 ? cv.x : cv.y;
    const float cf = (i & 1) ? hi16(cw) : lo16(cw);
    float s = cf * __builtin_amdgcn_rcpf(1.f + __builtin_amdgcn_exp2f(-1.4426950408889634f * cf));
    s = lo16(pk(s, s));
#pragma unroll
    for (int e = 0; e < 4; ++e) {
      if (i == 0) { a[2 * e] = s * lo16(wm[i][e]); a[2 * e + 1] = s * hi16(wm[i][e]); }
      else { a[2 * e] = __builtin_fmaf(s, lo16(wm[i][e]), a[2 * e]); a[2 * e + 1] = __builtin_fmaf(s, hi16(wm[i][e]), a[2 * e + 1]); }
    }
  }
  const float b0 = add16(a[0], a[1]), b1 = add16(a[2], a[3]), b2 = add16(a[4], a[5]), b3 = add16(a[6], a[7]);
  float c0 = add32(b0, b1), c1 = add32(b2, b3);
  c0 += dpp<DPP_ROR8>(c0); c1 += dpp<DPP_ROR8>(c1);
  c0 += dpp<DPP_ROR4>(c0); c1 += dpp<DPP_ROR4>(c1);
  if ((lane & 12) == 0) {
    LAS float* p = (LAS float*)(L + L_PART) + w * 32 + 8 * (lane & 3) + (lane >> 4);
    p[0] = c0; p[4] = c1;
  }
}
__device__ __forceinline__ void mod_publish(lptr L, char* ws, int strip, int lane, u64 tag) {
  if (lane < 32) {
    LAS float* p = (LAS float*)(L + L_PART) + lane;
    const float s = ((p[0] + p[32]) + (p[64] + p[96])) + ((p[128] + p[160]) + (p[192] + p[224]));
    const u64 g = (tag << 16) | (u64)(pk(s, s) & 0xffffu);
    __hip_atomic_store((GAS u64*)(ws + WS_MODG + (strip * 32 + lane) * 8), g, RLX_AGENT);
  }
}
__device__ __forceinline__ bool gran_ok(u32x4 g, u64 tag) {
  const u32 t0 = (u32)tag << 16, t1 = (u32)(tag >> 16);
  return (((g.x ^ t0) | (g.z ^ t0)) >> 16 == 0) && g.y == t1 && g.w == t1;
}
__device__ __forceinline__ u32 gran_val(u32x4 g, bool plus1) {
  if (plus1) return pk(1.f + lo16(g.x), 1.f + lo16(g.z));
  return (g.x & 0xffffu) | (g.z << 16);
}

#define MARK(i) do {} while (0)

__global__ void __launch_bounds__(512, 2)
fused_block(const u16* __restrict__ x, const u16* __restrict__ wqkv, const u16* __restrict__ wo, const u16* __restrict__ w1, const u16* __restrict__ b1,
            const u16* __restrict__ w2, const u16* __restrict__ b2, const u16* __restrict__ c, const u16* __restrict__ wmod, u16* __restrict__ out, char* ws) {
  __shared__ __attribute__((aligned(16))) char smem[LDS_BYTES];
  const lptr L = (lptr)smem;
  MARK(0);
  const int b = blockIdx.x, xcd = b & 7, bi = b >> 3;
  const int t = threadIdx.x, lane = t & 63, w = __builtin_amdgcn_readfirstlane(t >> 6);
  const int rbA = xcd * 4 + (bi >> 3), jA = bi & 7;
  const int rbB = (xcd >> 1) * 8 + (bi >> 2), jB = (xcd & 1) * 4 + (bi & 3);
  const int strip = 2 * (b & 7) + ((b >> 3) & 1) + 16 * (b >> 4);
  const u32x4 rsW = mkrs(ws, WS_END);
  const __amdgpu_buffer_rsrc_t rW = __builtin_amdgcn_make_buffer_rsrc(ws, 0, WS_END, 0x00020000);

  u32x2 ep, cv;
  u32x4 wm[4], xr[8];
  const u32 mvoff = (u32)(t >> 2) * (4 * 6144) + strip * 64 + (t & 3) * 16;
  {
    const u32x4 rsC = mkrs(c, b < 32 ? 1024u : 0u), rsM = mkrs(wmod, b < 32 ? 512u * 6144u : 0u);
    asm volatile("buffer_load_dwordx4 %0, %6, %7, 0 offen\n\t"
                 "buffer_load_dwordx4 %1, %6, %7, %8 offen\n\t"
                 "buffer_load_dwordx4 %2, %6, %7, %9 offen\n\t"
                 "buffer_load_dwordx4 %3, %6, %7, %10 offen\n\t"
                 "buffer_load_dwordx2 %4, %11, %12, 0 offen\n\t"
                 "buffer_load_dwordx2 %5, off, %13, 0 sc1"
                 : "=&v"(wm[0]), "=&v"(wm[1]), "=&v"(wm[2]), "=&v"(wm[3]), "=&v"(cv), "=&v"(ep)
                 : "v"(mvoff), "s"(rsM), "s"(6144), "s"(2 * 6144), "s"(3 * 6144), "v"((u32)(t >> 2) * 8), "s"(rsC), "s"(rsW)
                 : "memory");
  }
  {
    const u32x4 rsX = mkrs(x, 2048u * 1024u);
    const u32 so = (u32)(rbB * 64 + w * 8) * 1024;
    asm volatile("buffer_load_dwordx4 %0, %8, %9, %10 offen\n\t"
                 "buffer_load_dwordx4 %1, %8, %9, %10 offen offset:1024\n\t"
                 "buffer_load_dwordx4 %2, %8, %9, %10 offen offset:2048\n\t"
                 "buffer_load_dwordx4 %3, %8, %9, %10 offen offset:3072\n\t"
                 "buffer_load_dwordx4 %4, %8, %9, %11 offen\n\t"
                 "buffer_load_dwordx4 %5, %8, %9, %11 offen offset:1024\n\t"
                 "buffer_load_dwordx4 %6, %8, %9, %11 offen offset:2048\n\t"
                 "buffer_load_dwordx4 %7, %8, %9, %11 offen offset:3072"
                 : "=&v"(xr[0]), "=&v"(xr[1]), "=&v"(xr[2]), "=&v"(xr[3]), "=&v"(xr[4]), "=&v"(xr[5]), "=&v"(xr[6]), "=&v"(xr[7])
                 : "v"((u32)lane * 16), "s"(rsX), "s"(so), "s"(so + 4096)
                 : "memory");
  }
  const u32x4 rsQ = mkrs(wqkv, 512u * 3072u);
  const u32 wvoff = (u32)(lane >> 3) * 3072 + jB * 128 + (u32)((lane & 7) ^ (((lane >> 4) & 1) << 2)) * 16;
  dma8<8 * 3072>(8192 * w, wvoff, rsQ, 1024 + 64 * w * 3072);
  dma8<8 * 3072>(L_STG + 8192 * w, wvoff, rsQ, 2048 + 64 * w * 3072);
  {
    u64 pc;
    u32 len;
    asm volatile("s_getpc_b64 %0\n.Lpf_here:\n\ts_mov_b32 %1, .Lpf_end-.Lpf_here" : "=s"(pc), "=s"(len));
    if (w == 7) touch((u32)lane * 128, mkrs((const void*)pc, len), (u32)(bi & 3) * 8192);
    else touch((u32)(lane & 7) * 32768 + b * 128, rsW, WS_FLAG);
  }
  if (t == 0) { *(LAS u32x4*)(L + L_ONES) = u32x4{0x3f803f80u, 0x3f803f80u, 0x3f803f80u, 0x3f803f80u}; *(LAS u32x4*)(L + L_CNT) = u32x4{0, 0, 0, 0}; }
  asm volatile("s_waitcnt vmcnt(25)" : "+v"(wm[0]), "+v"(wm[1]), "+v"(wm[2]), "+v"(wm[3]), "+v"(cv), "+v"(ep)::"memory");
  const u64 tag = (((u64)(u32)__builtin_amdgcn_readfirstlane(ep.y) << 32) | (u32)__builtin_amdgcn_readfirstlane(ep.x)) + 1;
  MARK(1);
  if (b < 32) {
    mod_partial(L, cv, wm, w, lane);
    BAR();
    if (w == 0) mod_publish(L, ws, strip, lane, tag);
  }
  MARK(2);
  asm volatile("s_waitcnt vmcnt(17)" : "+v"(xr[0]), "+v"(xr[1]), "+v"(xr[2]), "+v"(xr[3]), "+v"(xr[4]), "+v"(xr[5]), "+v"(xr[6]), "+v"(xr[7])::"memory");
  float xn[8][8];
  {
    float s[8];
#pragma unroll
    for (int r = 0; r < 8; ++r) {
#pragma unroll
      for (int e = 0; e < 4; ++e) { xn[r][2 * e] = lo16(xr[r][e]); xn[r][2 * e + 1] = hi16(xr[r][e]); }
      s[r] = ((xn[r][0] + xn[r][1]) + (xn[r][2] + xn[r][3])) + ((xn[r][4] + xn[r][5]) + (xn[r][6] + xn[r][7]));
    }
    auto red8 = [&](float (&v)[8], float& c0, float& c1) {
      const float b0 = add16(v[0], v[1]), b1 = add16(v[2], v[3]), b2 = add16(v[4], v[5]), b3 = add16(v[6], v[7]);
      c0 = add32(b0, b1); c1 = add32(b2, b3);
      c0 += dpp<DPP_X1>(c0); c1 += dpp<DPP_X1>(c1); c0 += dpp<DPP_X2>(c0); c1 += dpp<DPP_X2>(c1);
      c0 += dpp<DPP_ROR4>(c0); c1 += dpp<DPP_ROR4>(c1); c0 += dpp<DPP_ROR8>(c0); c1 += dpp<DPP_ROR8>(c1);
    };
    float c0, c1;
    red8(s, c0, c1);
    c0 *= (1.f / 512.f); c1 *= (1.f / 512.f);
#pragma unroll
    for (int r = 0; r < 8; ++r) {
      const float m = asf(__builtin_amdgcn_readlane(asu(r < 4 ? c0 : c1), (r & 3) * 16));
      float q = 0.f;
#pragma unroll
      for (int e = 0; e < 8; ++e) { xn[r][e] -= m; q = __builtin_fmaf(xn[r][e], xn[r][e], q); }
      s[r] = q;
    }
    red8(s, c0, c1);
    c0 = __builtin_amdgcn_rsqf(__builtin_fmaf(c0, 1.f / 512.f, 1e-5f)); c1 = __builtin_amdgcn_rsqf(__builtin_fmaf(c1, 1.f / 512.f, 1e-5f));
#pragma unroll
    for (int r = 0; r < 8; ++r) {
      const float rs = asf(__builtin_amdgcn_readlane(asu(r < 4 ? c0 : c1), (r & 3) * 16));
#pragma unroll
      for (int e = 0; e < 8; ++e) xn[r][e] *= rs;
    }
  }
  MARK(3);
  VMCNT(0);
  BAR();
  MARK(4);
  const int pr = w & 1, th = (w >> 1) & 1;
  const int G = lane >> 4, q4 = (lane >> 2) & 3, pp = lane & 3;
  const u32 wtr = (u32)(8 * (G >> 1) + q4) * 128 + (u32)((64 * pr) ^ (64 * ((q4 >> 1) & 1))) + 32 * (G & 1) + 8 * pp;
  u32x4 wf[32];
  {
    u32x4 g = __builtin_amdgcn_raw_buffer_load_b128(rW, WS_MODG + t * 16, 0, 16);
    const lptr p = L + (w < 4 ? 0u : L_STG) + wtr;
#pragma unroll
    for (int ks = 0; ks < 32; ++ks) wf[ks] = trfrag(p, ks * 2048, ks * 2048 + 512);
    SCHED();
    for (int s = 0; s < SPIN_CAP; ++s) {
      if (__all(gran_ok(g, tag))) break;
      asm volatile("" ::: "memory");
      g = __builtin_amdgcn_raw_buffer_load_b128(rW, WS_MODG + t * 16, 0, 16);
    }
    *(LAS u32*)(L + L_MOD + 4 * t) = gran_val(g, w >= 4);
  }
  BAR();
  MARK(5);
  if (w < 4) dma16<8 * 3072>(L_STG + 16384 * w, wvoff, rsQ, 128 * w * 3072);
  {
    const u32x4 sa = lds128(L + L_MOD + lane * 16), ga = lds128(L + L_MOD + 1024 + lane * 16);
    const lptr hp = L + ((w >> 2) ? L_VIMG : L_KIMG) + lane * 512;
#pragma unroll
    for (int r = 0; r < 8; ++r) {
      u32x4 o;
#pragma unroll
      for (int e = 0; e < 4; ++e)
        o[e] = pk(__builtin_fmaf(xn[r][2 * e], lo16(ga[e]), lo16(sa[e])), __builtin_fmaf(xn[r][2 * e + 1], hi16(ga[e]), hi16(sa[e])));
      *(LAS u32x4*)(hp + ((8 * (w & 3) + r) ^ (lane & 7)) * 16) = o;
    }
  }
  const bool late = b >= 32 && b < 96;
  {
    const u32x4 rsC = mkrs(c, late ? 1024u : 0u), rsM = mkrs(wmod, late ? 512u * 6144u : 0u);
    asm volatile("buffer_load_dwordx4 %0, %5, %6, 0 offen\n\t"
                 "buffer_load_dwordx4 %1, %5, %6, %7 offen\n\t"
                 "buffer_load_dwordx4 %2, %5, %6, %8 offen\n\t"
                 "buffer_load_dwordx4 %3, %5, %6, %9 offen\n\t"
                 "buffer_load_dwordx2 %4, %10, %11, 0 offen"
                 : "=&v"(wm[0]), "=&v"(wm[1]), "=&v"(wm[2]), "=&v"(wm[3]), "=&v"(cv)
                 : "v"(mvoff), "s"(rsM), "s"(6144), "s"(2 * 6144), "s"(3 * 6144), "v"((u32)(t >> 2) * 8), "s"(rsC)
                 : "memory");
  }
  BAR();
  MARK(6);

  const int r32 = lane & 31, hh = lane >> 5;
  const int slotB = kvslot(rbB, jB), idB = rbB * 8 + jB, idA = rbA * 8 + jA;
  lptr hb[4];
#pragma unroll
  for (int i = 0; i < 4; ++i) hb[i] = L + (th ? L_VIMG : L_KIMG) + hh * 512 + (r32 ^ ((2 * i + hh) & 7)) * 16;
  {
    f32x16 acc = {};
    u32x4 o0, o1;
    u32 go0, go1, lo0, lo1;
    if (w < 4) {
      __builtin_amdgcn_s_setprio(2);
#pragma unroll
      for (int ks = 0; ks < 32; ++ks) acc = MFMA32(wf[ks], lds128(hb[ks & 3] + ks * 1024), acc);
      __builtin_amdgcn_s_setprio(0);
      lo0 = (th * 4 + 2 * pr) * 1024 + lane * 16; lo1 = lo0 + 1024;
      go0 = WS_K + slotB * 8192 + lo0; go1 = go0 + 1024;
      lo0 += L_KIMG + 32768; lo1 += L_KIMG + 32768;
    } else {
      __builtin_amdgcn_s_setprio(1);
#pragma unroll
      for (int ks = 0; ks < 32; ++ks) acc = MFMA32(lds128(hb[ks & 3] + ks * 1024), wf[ks], acc);
      __builtin_amdgcn_s_setprio(0);
      lo0 = (th * 4 + 2 * pr + (r32 >> 4)) * 1024 + hh * 256 + (lane & 15) * 16; lo1 = lo0 + 512;
      go0 = WS_V + slotB * 8192 + lo0; go1 = go0 + 512;
      lo0 += L_VIMG + 32768; lo1 += L_VIMG + 32768;
    }
#pragma unroll
    for (int e = 0; e < 4; ++e) { o0[e] = pk(acc[2 * e], acc[2 * e + 1]); o1[e] = pk(acc[8 + 2 * e], acc[8 + 2 * e + 1]); }
    __builtin_amdgcn_raw_buffer_store_b128(o0, rW, go0, 0, 16);
    __builtin_amdgcn_raw_buffer_store_b128(o1, rW, go1, 0, 16);
    *(LAS u32x4*)(L + lo0) = o0;
    *(LAS u32x4*)(L + lo1) = o1;
  }
  MARK(7);
  asm volatile("s_waitcnt vmcnt(2)" : "+v"(wm[0]), "+v"(wm[1]), "+v"(wm[2]), "+v"(wm[3]), "+v"(cv)::"memory");
  if (late) mod_partial(L, cv, wm, w, lane);
  VMCNT(0);
  if (w < 4) {
    LAS u32* cnt = (LAS u32*)(L + L_CNT);
    u32 old = 0;
    if (lane == 0) old = __hip_atomic_fetch_add(cnt, 1u, RLX_WG);
    old = __builtin_amdgcn_readfirstlane(old);
    if (old == 3) { if (lane == 0) set_flag(ws, F_K, idB, tag); }
    else { for (int s = 0; s < SPIN_CAP; ++s) { if (__hip_atomic_load(cnt, RLX_WG) >= 4u) break; __builtin_amdgcn_s_sleep(1); } }
    asm volatile("" ::: "memory");
    f32x16 acc = {};
    const lptr p = L + L_STG + wtr;
#pragma unroll
    for (int ks = 0; ks < 32; ++ks) acc = MFMA32(trfrag(p, ks * 2048, ks * 2048 + 512), lds128(hb[ks & 3] + ks * 1024), acc);
    u32x4 o0, o1;
#pragma unroll
    for (int e = 0; e < 4; ++e) { o0[e] = pk(acc[2 * e] * QSCALE, acc[2 * e + 1] * QSCALE); o1[e] = pk(acc[8 + 2 * e] * QSCALE, acc[8 + 2 * e + 1] * QSCALE); }
    *(LAS u32x4*)(L + L_Q + (th * 4 + 2 * pr) * 1024 + lane * 16) = o0;
    *(LAS u32x4*)(L + L_Q + (th * 4 + 2 * pr + 1) * 1024 + lane * 16) = o1;
  }
  BAR();
  MARK(8);
  const u32x4 rs1 = mkrs(w1, w < 7 ? 512u * 3584u : 0u);
  const int st1 = (jA & 1) ? 6 - w : w;
  const u32 w1voff = (u32)(lane >> 2) * 3584 + jA * 448 + st1 * 64 + (u32)((lane & 3) ^ (((lane >> 5) & 1) << 1)) * 16;
  const u32 stg1 = L_STG + w * 8192;
  auto w1_issue = [&](int round) { dma8<16 * 3584>(stg1, w1voff, rs1, 128 * round * 3584); };
  u32x4 w1f[2][16];
  const lptr w1p0 = L + stg1 + (8 * G + q4) * 64 + (2 * (0 ^ (G & 1)) + (pp >> 1)) * 16 + 8 * (pp & 1);
  const lptr w1p1 = L + stg1 + (8 * G + q4) * 64 + (2 * (1 ^ (G & 1)) + (pp >> 1)) * 16 + 8 * (pp & 1);
#define W1_READ(round)                                                                                        \
  _Pragma("unroll") for (int k_ = 0; k_ < 4; ++k_) {                                                          \
    w1f[0][4 * (round) + k_] = trfrag(w1p0, k_ * 2048, k_ * 2048 + 256);                                      \
    w1f[1][4 * (round) + k_] = trfrag(w1p1, k_ * 2048, k_ * 2048 + 256);                                      \
  }                                                                                                           \
  asm volatile("s_waitcnt lgkmcnt(0)" ::: "memory")
  {
    const int prb = rbB - 4 + (w & 3);
    if (w == 4 && lane == 0) set_flag(ws, F_V, idB, tag);
    if (w == 5 && late) mod_publish(L, ws, strip, lane, tag);
    if (w >= 4) w1_issue(0);
    if (prb >= 0) {
      poll(ws, flag_off(w < 4 ? F_K : F_V, prb * 8 + jB), tag);
      const u32 so = (w < 4 ? WS_K : WS_V) + kvslot(prb, jB) * 8192, ld = (w < 4 ? L_KIMG : L_VIMG) + (w & 3) * 8192;
      dma4_sc1(ld, lane * 16, rsW, so);
      dma4_sc1(ld + 4096, lane * 16, rsW, so + 4096);
    }
    if (w < 4) { w1_issue(0); VMCNT(8); }
  }
  BAR();
  MARK(9);

  {
    const int head = w & 3, qh = w >> 2;
    const int istart = 8 - 2 * rbB - qh;
    const u32x4 qf = lds128(L + L_Q + (qh * 4 + head) * 1024 + lane * 16);
    const lptr kp = L + L_KIMG + (qh * 4 + head) * 1024 + lane * 16;
    const f32x16 zero = {};
    f32x16 s08 = MFMA32(lds128(kp + 8 * 4096), qf, zero);
    {
      f32x16 s0;
      if (istart <= 0) s0 = MFMA32(lds128(kp), qf, zero);
      else {
#pragma unroll
        for (int v = 0; v < 16; ++v) s0[v] = -INFINITY;
      }
#pragma unroll
      for (int v = 0; v < 16; ++v) s08[v] = ((v & 3) + 8 * (v >> 2) + 4 * hh <= r32) ? s08[v] : s0[v];
    }
    float m = s08[0];
#pragma unroll
    for (int v = 1; v < 16; ++v) m = __builtin_fmaxf(m, s08[v]);
    const int i1 = istart > 1 ? istart : 1;
    for (int i = i1; i < 8; ++i) {
      const f32x16 s = MFMA32(lds128(kp + i * 4096), qf, zero);
#pragma unroll
      for (int v = 0; v < 16; ++v) m = __builtin_fmaxf(m, s[v]);
    }
    { auto r = __builtin_amdgcn_permlane32_swap(asu(m), asu(m), false, false); m = __builtin_fmaxf(asf(r[0]), asf(r[1])); }
    VMCNT(0);
    BAR();
    MARK(10);
    W1_READ(0);
    w1_issue(1);
    const bool vl = r32 < 16;
    const u32 vbase = vl ? L_VIMG + (qh * 4 + head) * 1024 + hh * 256 + r32 * 16 : L_ONES, vts = vl ? 4096u : 0u, vss = vl ? 512u : 0u;
    f32x16 acc;
    {
      u32x4 p0, p1, a0, a1;
#pragma unroll
      for (int e = 0; e < 4; ++e) {
        p0[e] = pk(__builtin_amdgcn_exp2f(s08[2 * e] - m), __builtin_amdgcn_exp2f(s08[2 * e + 1] - m));
        p1[e] = pk(__builtin_amdgcn_exp2f(s08[8 + 2 * e] - m), __builtin_amdgcn_exp2f(s08[8 + 2 * e + 1] - m));
      }
      u32x4 mk0, mk1;
#pragma unroll
      for (int e = 0; e < 4; ++e) {
        const int k0 = ((2 * e) & 3) + 8 * ((2 * e) >> 2) + 4 * hh;
        mk0[e] = (k0 <= r32 ? 0xffffu : 0u) | (k0 + 1 <= r32 ? 0xffff0000u : 0u);
        mk1[e] = (k0 + 16 <= r32 ? 0xffffu : 0u) | (k0 + 17 <= r32 ? 0xffff0000u : 0u);
      }
      a0 = p0 & mk0; a1 = p1 & mk1;
      acc = MFMA32(lds128(L + vbase + 8 * vts), a0, zero);
      acc = MFMA32(lds128(L + vbase + 8 * vts + vss), a1, acc);
      if (istart <= 0) {
        a0 = p0 & ~mk0; a1 = p1 & ~mk1;
        acc = MFMA32(lds128(L + vbase), a0, acc);
        acc = MFMA32(lds128(L + vbase + vss), a1, acc);
      }
    }
    f32x16 nm;
#pragma unroll
    for (int v = 0; v < 16; ++v) nm[v] = -m;
    for (int i = i1; i < 8; ++i) {
      const f32x16 s = MFMA32(lds128(kp + i * 4096), qf, nm);
      u32x4 p0, p1;
#pragma unroll
      for (int e = 0; e < 4; ++e) {
        p0[e] = pk(__builtin_amdgcn_exp2f(s[2 * e]), __builtin_amdgcn_exp2f(s[2 * e + 1]));
        p1[e] = pk(__builtin_amdgcn_exp2f(s[8 + 2 * e]), __builtin_amdgcn_exp2f(s[8 + 2 * e + 1]));
      }
      acc = MFMA32(lds128(L + vbase + i * vts), p0, acc);
      acc = MFMA32(lds128(L + vbase + i * vts + vss), p1, acc);
    }
    const float rl = __builtin_amdgcn_rcpf(acc[8]);
    u32x4 o;
#pragma unroll
    for (int e = 0; e < 4; ++e) o[e] = pk(acc[2 * e] * rl, acc[2 * e + 1] * rl);
    const u32 go = WS_O + ((rbB * 4 + 2 * qh + (r32 >> 4)) * 16 + 2 * jB + (head >> 1)) * 1024 + ((2 * (head & 1) + hh) * 16 + (lane & 15)) * 16;
    __builtin_amdgcn_raw_buffer_store_b128(o, rW, go, 0, 16);
  }
  MARK(11);
  VMCNT(0);
  BAR();
  if (t == 0) set_flag(ws, F_OA, idB, tag);
  MARK(12);
  W1_READ(1);
  {
    const u32x4 rsO = mkrs(wo, 512u * 1024u);
    const u32 vo = (u32)(lane >> 3) * 1024 + jA * 128 + (u32)((lane & 7) ^ (((lane >> 4) & 3) << 1)) * 16;
    dma8<8 * 1024>(L_WO + 8192 * w, vo, rsO, 64 * w * 1024);
  }
  w1_issue(2);
  const int mt = w >> 1, kh = w & 1, tok = lane & 15;
  const int col0 = 64 * jA + 32 * kh + 16 * (G & 1) + 8 * (G >> 1);
  const u32x4 xres = *(const GAS u32x4*)(x + (size_t)(rbA * 64 + mt * 16 + tok) * 512 + col0);
  for (int s = 0; s < SPIN_CAP; ++s) {
    asm volatile("" ::: "memory");
    const u32x4 g0 = __builtin_amdgcn_raw_buffer_load_b128(rW, WS_MODG + 8192 + t * 16, 0, 16);
    const u32x4 g1 = __builtin_amdgcn_raw_buffer_load_b128(rW, WS_MODG + 16384 + t * 16, 0, 16);
    *(LAS u32*)(L + L_MOD + 2048 + 4 * t) = gran_val(g0, false);
    *(LAS u32*)(L + L_MOD + 4096 + 4 * t) = gran_val(g1, w < 4);
    if (__all(gran_ok(g0, tag) && gran_ok(g1, tag))) break;
  }
  VMCNT(0);
  W1_READ(2);
  w1_issue(3);
  BAR();
  MARK(13);

  u32x4 x1p;
  {
    poll(ws, flag_off(F_OA, rbA * 8 + 4 * kh + (lane & 3)), tag);
    MARK(14);
    u32x4 of[8];
#pragma unroll
    for (int k = 0; k < 8; ++k) of[k] = __builtin_amdgcn_raw_buffer_load_b128(rW, WS_O + ((rbA * 4 + mt) * 16 + 8 * kh + k) * 1024 + lane * 16, 0, 16);
    f32x4 acc[4] = {};
    const int sw = 2 * (G & 1) + (q4 >> 1);
    const lptr wb = L + L_WO + (256 * kh + 16 * (G >> 1) + 4 * (G & 1) + q4) * 128 + (pp >> 1) * 16 + 8 * (pp & 1);
#pragma unroll
    for (int k = 0; k < 8; ++k)
#pragma unroll
      for (int nt = 0; nt < 4; ++nt) acc[nt] = MFMA16(trfrag(wb + (nt ^ sw) * 32, k * 4096, k * 4096 + 1024), of[k], acc[nt]);
    *(LAS f32x4*)(L + L_XCH + (w * 2 + 0) * 1024 + lane * 16) = kh ? acc[0] : acc[2];
    *(LAS f32x4*)(L + L_XCH + (w * 2 + 1) * 1024 + lane * 16) = kh ? acc[1] : acc[3];
    BAR();
    MARK(15);
    f32x4 t0 = (kh ? acc[2] : acc[0]) + *(LAS f32x4*)(L + L_XCH + ((w ^ 1) * 2 + 0) * 1024 + lane * 16);
    f32x4 t1 = (kh ? acc[3] : acc[1]) + *(LAS f32x4*)(L + L_XCH + ((w ^ 1) * 2 + 1) * 1024 + lane * 16);
    float y[8];
#pragma unroll
    for (int r = 0; r < 4; ++r) { auto s = __builtin_amdgcn_permlane16_swap(asu(t0[r]), asu(t1[r]), false, false); y[r] = asf(s[0]); y[4 + r] = asf(s[1]); }
    const u32x4 ga = lds128(L + L_MOD + 2048 + col0 * 2), g2 = lds128(L + L_MOD + 4096 + col0 * 2);
    u32x4 xg;
    float s1 = 0.f, s2 = 0.f;
#pragma unroll
    for (int e = 0; e < 4; ++e) {
      x1p[e] = pk(__builtin_fmaf(lo16(ga[e]), y[2 * e], lo16(xres[e])), __builtin_fmaf(hi16(ga[e]), y[2 * e + 1], hi16(xres[e])));
      const float a = lo16(x1p[e]), bb = hi16(x1p[e]);
      xg[e] = pk(a * lo16(g2[e]), bb * hi16(g2[e]));
      s1 += a + bb; s2 = __builtin_fmaf(a, a, __builtin_fmaf(bb, bb, s2));
    }
    __builtin_amdgcn_raw_buffer_store_b128(xg, rW, WS_X + ((rbA * 4 + mt) * 16 + 2 * jA + kh) * 1024 + ((2 * (G & 1) + (G >> 1)) * 16 + tok) * 16, 0, 16);
    float z = add16(s1, s2);
    z = add32(z, z);
    if (lane < 32) __builtin_amdgcn_raw_buffer_store_b32(asu(z), rW, WS_ST + ((rbA * 4 + mt) * 16 + 2 * jA + kh) * 128 + lane * 4, 0, 16);
  }
  VMCNT(0);
  BAR();
  if (t == 0) set_flag(ws, F_XA, idA, tag);
  MARK(16);
  W1_READ(3);
  float uu[8], vv[8];
  {
    f32x4 a0 = {}, a1 = {};
    const lptr p = L + L_MOD + ((tok & 1) ? 3072 : 4096) + G * 16;
#pragma unroll
    for (int ks = 0; ks < 16; ++ks) { const u32x4 f = lds128(p + ks * 64); a0 = MFMA16(w1f[0][ks], f, a0); a1 = MFMA16(w1f[1][ks], f, a1); }
    const int n0 = 224 * jA + 32 * (w < 7 ? st1 : 0) + 4 * G;
    const u32x2 bb0 = *(const GAS u32x2*)(b1 + n0), bb1 = *(const GAS u32x2*)(b1 + n0 + 16);
#pragma unroll
    for (int r = 0; r < 4; ++r) {
      uu[r] = dpp<0xA0>(a0[r]); uu[4 + r] = dpp<0xA0>(a1[r]);
      vv[r] = dpp<0xF5>(a0[r]) + ((r & 1) ? hi16(bb0[r >> 1]) : lo16(bb0[r >> 1]));
      vv[4 + r] = dpp<0xF5>(a1[r]) + ((r & 1) ? hi16(bb1[r >> 1]) : lo16(bb1[r >> 1]));
    }
  }
  poll(ws, flag_off(F_XA, rbA * 8 + (w == 7 ? (lane & 7) : w)), tag);
  MARK(17);
#pragma unroll
  for (int m = 0; m < 4; ++m) dma2_sc1(L_X1 + (m * 16 + 2 * w) * 1024, lane * 16, rsW, WS_X + ((rbA * 4 + m) * 16 + 2 * w) * 1024);
  if (w == 7) {
    u32x4 sv[8];
#pragma unroll
    for (int k = 0; k < 8; ++k) sv[k] = __builtin_amdgcn_raw_buffer_load_b128(rW, WS_ST + rbA * 8192 + k * 1024 + lane * 16, 0, 16);
#pragma unroll
    for (int m = 0; m < 4; ++m) {
      float a[4];
#pragma unroll
      for (int e = 0; e < 4; ++e) a[e] = asf(sv[2 * m][e]) + asf(sv[2 * m + 1][e]);
      float z = add32(add16(a[0], a[1]), add16(a[2], a[3]));
      z += dpp<DPP_ROR8>(z);
      const float o = dpp<DPP_ROR4>(z);
      const float mean = z * (1.f / 512.f);
      const float var = __builtin_fmaxf(__builtin_fmaf(o, 1.f / 512.f, -mean * mean), 0.f);
      const float rs = __builtin_amdgcn_rsqf(var + 1e-5f);
      if ((lane & 12) == 0) *(LAS f32x2*)(L + L_STAT + (m * 16 + 4 * (lane & 3) + G) * 8) = f32x2{rs, -mean * rs};
    }
  }
  VMCNT(0);
  BAR();
  MARK(18);

  if (w < 7) {
#pragma unroll
    for (int half = 0; half < 2; ++half) {
      f32x4 acc[2][2] = {};
      const lptr xp = L + L_X1 + half * 32768 + lane * 16;
#pragma unroll
      for (int ks = 0; ks < 16; ++ks)
#pragma unroll
        for (int m = 0; m < 2; ++m) {
          const u32x4 f = lds128(xp + m * 16384 + ks * 1024);
          acc[m][0] = MFMA16(w1f[0][ks], f, acc[m][0]);
          acc[m][1] = MFMA16(w1f[1][ks], f, acc[m][1]);
        }
#pragma unroll
      for (int m = 0; m < 2; ++m) {
        const f32x2 st = *(LAS f32x2*)(L + L_STAT + ((2 * half + m) * 16 + tok) * 8);
        float hv[8];
#pragma unroll
        for (int e = 0; e < 8; ++e) {
          float v = __builtin_fmaf(st.x, acc[m][e >> 2][e & 3], __builtin_fmaf(st.y, uu[e], vv[e]));
          v = v > 0.f ? v : 0.f;
          hv[e] = v * v;
        }
        const u32x4 o = {pk(hv[0], hv[1]), pk(hv[2], hv[3]), pk(hv[4], hv[5]), pk(hv[6], hv[7])};
        __builtin_amdgcn_raw_buffer_store_b128(o, rW, WS_H + ((rbA * 4 + 2 * half + m) * 56 + 7 * jA + st1) * 1024 + lane * 16, 0, 16);
      }
    }
  } else if (b == 0) {
#pragma unroll
    for (int k = 0; k < 4; ++k) poll(ws, flag_off(F_K, k * 64 + lane), tag);
    if (lane == 0) __hip_atomic_store((GAS u64*)(ws + WS_EPOCH), tag, RLX_AGENT);
  }
  MARK(19);
  VMCNT(0);
  BAR();
  if (t == 0) set_flag(ws, F_HA, idA, tag);
  MARK(20);

  if (w < 7) {
    const u32x4 rs2 = mkrs(w2, 1792u * 1024u);
    const u32 vo = (u32)(lane >> 3) * 1024 + jA * 128 + (u32)((lane & 7) ^ (((lane >> 4) & 3) << 1)) * 16;
    const u32 stg2 = w * 16384;
    u32x4 w2f[8][4];
    const int sw = 2 * (G & 1) + (q4 >> 1);
    const lptr wb = L + stg2 + (4 * G + q4) * 128 + (pp >> 1) * 16 + 8 * (pp & 1);
#pragma unroll
    for (int rd = 0; rd < 2; ++rd) {
      dma16<8 * 1024>(stg2, vo, rs2, (256 * w + 128 * rd) * 1024);
      VMCNT(0);
#pragma unroll
      for (int k = 0; k < 4; ++k)
#pragma unroll
        for (int nt = 0; nt < 4; ++nt) w2f[4 * rd + k][nt] = trfrag(wb + (nt ^ sw) * 32, k * 4096, k * 4096 + 2048);
      asm volatile("s_waitcnt lgkmcnt(0)" ::: "memory");
    }
    poll(ws, flag_off(F_HA, rbA * 8 + (lane & 7)), tag);
    MARK(21);
#pragma unroll
    for (int m = 0; m < 4; ++m) {
      u32x4 hf[8];
#pragma unroll
      for (int k = 0; k < 8; ++k) hf[k] = __builtin_amdgcn_raw_buffer_load_b128(rW, WS_H + ((rbA * 4 + m) * 56 + 8 * w + k) * 1024 + lane * 16, 0, 16);
      f32x4 acc[4] = {};
#pragma unroll
      for (int k = 0; k < 8; ++k)
#pragma unroll
        for (int nt = 0; nt < 4; ++nt) acc[nt] = MFMA16(w2f[k][nt], hf[k], acc[nt]);
#pragma unroll
      for (int nt = 0; nt < 4; ++nt) *(LAS f32x4*)(L + stg2 + (m * 4 + nt) * 1024 + lane * 16) = acc[nt];
    }
  }
  const u32x4 b2v = *(const GAS u32x4*)(b2 + col0);
  BAR();
  MARK(22);
  {
    const lptr p = L + (mt * 4 + 2 * kh + (G & 1)) * 1024 + ((2 * (G >> 1)) * 16 + tok) * 16;
    f32x4 s0 = *(LAS f32x4*)p, s1 = *(LAS f32x4*)(p + 256);
#pragma unroll
    for (int v = 1; v < 7; ++v) { s0 += *(LAS f32x4*)(p + v * 16384); s1 += *(LAS f32x4*)(p + v * 16384 + 256); }
    const u32x4 gb = lds128(L + L_MOD + 5120 + col0 * 2);
    u32x4 o;
#pragma unroll
    for (int e = 0; e < 4; ++e) {
      const float a = e < 2 ? s0[2 * e] : s1[2 * e - 4], bb = e < 2 ? s0[2 * e + 1] : s1[2 * e - 3];
      o[e] = pk(__builtin_fmaf(lo16(gb[e]), a + lo16(b2v[e]), lo16(x1p[e])), __builtin_fmaf(hi16(gb[e]), bb + hi16(b2v[e]), hi16(x1p[e])));
    }
    *(GAS u32x4*)(out + (size_t)(rbA * 64 + mt * 16 + tok) * 512 + col0) = o;
  }
  asm volatile(".Lpf_end:");
}
}

extern "C" void kernel_launch(void* const* d_in, const int* in_sizes, int n_in, void* d_out, int out_size, void* d_ws, size_t ws_size, hipStream_t stream) {
  fused_block<<<256, 512, 0, stream>>>((const u16*)d_in[0], (const u16*)d_in[1], (const u16*)d_in[2], (const u16*)d_in[4], (const u16*)d_in[5], (const u16*)d_in[6],
                                       (const u16*)d_in[7], (const u16*)d_in[8], (const u16*)d_in[9], (u16*)d_out, (char*)d_ws);
}
```

```cpp
#include <hip/hip_runtime.h>
#include <hip/hip_bf16.h>
#include <stdint.h>

#define PROF 0
#define MAPB 1

namespace {
typedef unsigned short u16;
typedef unsigned u32;
typedef unsigned long long u64;
typedef float f32x2 __attribute__((ext_vector_type(2)));
typedef float f32x4 __attribute__((ext_vector_type(4)));
typedef float f32x16 __attribute__((ext_vector_type(16)));
typedef u32 u32x2 __attribute__((ext_vector_type(2)));
typedef u32 u32x4 __attribute__((ext_vector_type(4)));
typedef short bf16x8 __attribute__((ext_vector_type(8)));
typedef short s16x4 __attribute__((ext_vector_type(4)));
typedef __bf16 bf16x2_t __attribute__((ext_vector_type(2)));
#define GAS __attribute__((address_space(1)))
#define LAS __attribute__((address_space(3)))
#define RLX_AGENT __ATOMIC_RELAXED, __HIP_MEMORY_SCOPE_AGENT
#define RLX_WG __ATOMIC_RELAXED, __HIP_MEMORY_SCOPE_WORKGROUP

constexpr u32 WS_EPOCH = 0, WS_MODG = 4096, WS_FLAG = 32768;
constexpr u32 WS_K = 1u << 20, WS_V = 3u << 20, WS_O = 5u << 20, WS_X = 7u << 20, WS_ST = 9u << 20, WS_H = 10u << 20, WS_PROF = 18u << 20;
constexpr u32 WS_W1T = 20u << 20, WS_WOT = WS_W1T + 1792u * 1024u, WS_W2T = WS_WOT + 512u * 1024u, WS_END = 24u << 20;
enum { F_K = 0, F_V = 1, F_O = 2, F_W = 3, F_X = 4, F_HA = 6, F_HB = 7 };
constexpr u32 L_H = 0, L_STG = 65536, L_RT = 131072;
constexpr u32 L_KIMG = 0, L_VIMG = 40960, L_Q = 81920, L_WO = 90112;
constexpr u32 L_XCH = 0, L_X1 = 16384, L_W2 = 90112, L_PB = 0;
constexpr u32 L_MOD = 155648, L_MISC = 161792;
constexpr u32 L_PART = L_MISC, L_STAT = L_MISC + 1024, L_DUMMY = L_MISC + 1536, L_ONES = L_MISC + 1792, L_CNT = L_MISC + 1808;
enum { C_K = 0, C_WQ, C_XB, C_HA, C_HB, C_STAT, C_RA, C_RB, C_MOD, C_WK, C_WV, C_OWN, C_KH, C_VH, C_WF, C_O };
constexpr u32 LDS_BYTES = 163840;
constexpr int SPIN_CAP = 1 << 18;
constexpr float QSCALE = 0.25f * 1.4426950408889634f;

__device__ __forceinline__ u32 pk(float a, float b) { f32x2 v = {a, b}; return __builtin_bit_cast(u32, __builtin_convertvector(v, bf16x2_t)); }
__device__ __forceinline__ float lo16(u32 v) { return __builtin_bit_cast(float, v << 16); }
__device__ __forceinline__ float hi16(u32 v) { return __builtin_bit_cast(float, v & 0xffff0000u); }
__device__ __forceinline__ float asf(u32 v) { return __builtin_bit_cast(float, v); }
__device__ __forceinline__ u32 asu(float v) { return __builtin_bit_cast(u32, v); }
template <int CTRL> __device__ __forceinline__ float dpp(float v) { return asf((u32)__builtin_amdgcn_update_dpp(0, (int)asu(v), CTRL, 0xF, 0xF, true)); }
__device__ __forceinline__ float add16(float a, float b) { auto r = __builtin_amdgcn_permlane16_swap(asu(a), asu(b), false, false); return asf(r[0]) + asf(r[1]); }
__device__ __forceinline__ float add32(float a, float b) { auto r = __builtin_amdgcn_permlane32_swap(asu(a), asu(b), false, false); return asf(r[0]) + asf(r[1]); }
constexpr int DPP_X1 = 0xB1, DPP_X2 = 0x4E, DPP_HMIR = 0x141, DPP_MIR = 0x140, DPP_ROR4 = 0x124, DPP_ROR8 = 0x128;

__device__ __forceinline__ u32x4 mkrs(const void* p, u32 bytes) { return u32x4{(u32)(uintptr_t)p, (u32)((uintptr_t)p >> 32), bytes, 0x00020000u}; }
#define DMA_L(o) "buffer_load_dwordx4 %2, %3, %0 offen offset:" #o " lds\n\t"
#define DMA_A "s_add_u32 %0, %0, %4\n\t"
#define DMA_G DMA_L(0) DMA_A DMA_L(1024) DMA_A DMA_L(2048) DMA_A DMA_L(3072)
#define DMA_N "s_add_u32 m0, m0, 0x1000\n\ts_add_u32 %0, %0, %5\n\t"
template <int STRIDE> __device__ __forceinline__ void dma4(u32 lds, u32 voff, u32x4 rs, u32 soff) {
  asm volatile("s_mov_b32 m0, %1\n\ts_nop 0\n\t" DMA_G : "+&s"(soff) : "s"(lds), "v"(voff), "s"(rs), "s"(STRIDE - 1024), "i"(STRIDE + 3072) : "memory", "scc");
}
template <int STRIDE> __device__ __forceinline__ void dma8(u32 lds, u32 voff, u32x4 rs, u32 soff) {
  asm volatile("s_mov_b32 m0, %1\n\ts_nop 0\n\t" DMA_G DMA_N DMA_G : "+&s"(soff) : "s"(lds), "v"(voff), "s"(rs), "s"(STRIDE - 1024), "i"(STRIDE + 3072) : "memory", "scc");
}
template <int STRIDE> __device__ __forceinline__ void dma16(u32 lds, u32 voff, u32x4 rs, u32 soff) {
  asm volatile("s_mov_b32 m0, %1\n\ts_nop 0\n\t" DMA_G DMA_N DMA_G DMA_N DMA_G DMA_N DMA_G : "+&s"(soff) : "s"(lds), "v"(voff), "s"(rs), "s"(STRIDE - 1024), "i"(STRIDE + 3072) : "memory", "scc");
}
__device__ __forceinline__ void dma4_sc1(u32 lds, u32 voff, u32x4 rs, u32 soff) {
  asm volatile("s_mov_b32 m0, %0\n\ts_nop 0\n\t"
               "buffer_load_dwordx4 %1, %2, %3 offen sc1 lds\n\t"
               "buffer_load_dwordx4 %1, %2, %3 offen offset:1024 sc1 lds\n\t"
               "buffer_load_dwordx4 %1, %2, %3 offen offset:2048 sc1 lds\n\t"
               "buffer_load_dwordx4 %1, %2, %3 offen offset:3072 sc1 lds" ::"s"(lds), "v"(voff), "s"(rs), "s"(soff) : "memory");
}
__device__ __forceinline__ void dma2_sc1(u32 lds, u32 voff, u32x4 rs, u32 soff) {
  asm volatile("s_mov_b32 m0, %0\n\ts_nop 0\n\t"
               "buffer_load_dwordx4 %1, %2, %3 offen sc1 lds\n\t"
               "buffer_load_dwordx4 %1, %2, %3 offen offset:1024 sc1 lds" ::"s"(lds), "v"(voff), "s"(rs), "s"(soff) : "memory");
}
__device__ __forceinline__ void touch(u32 voff, u32x4 rs, u32 soff) {
  asm volatile("s_mov_b32 m0, %0\n\ts_nop 0\n\tbuffer_load_dword %1, %2, %3 offen lds" ::"s"(L_DUMMY), "v"(voff), "s"(rs), "s"(soff) : "memory");
}
#define VMCNT(n) asm volatile("s_waitcnt vmcnt(" #n ")" ::: "memory")
#define BAR() asm volatile("s_waitcnt lgkmcnt(0)\n\ts_barrier" ::: "memory")
#define SCHED() __builtin_amdgcn_sched_barrier(0)
#define MFMA32(a, b, c) __builtin_amdgcn_mfma_f32_32x32x16_bf16(__builtin_bit_cast(bf16x8, a), __builtin_bit_cast(bf16x8, b), c, 0, 0, 0)
#define MFMA16(a, b, c) __builtin_amdgcn_mfma_f32_16x16x32_bf16(__builtin_bit_cast(bf16x8, a), __builtin_bit_cast(bf16x8, b), c, 0, 0, 0)

typedef LAS char* lptr;
__device__ __forceinline__ u32x4 lds128(lptr p) { return *(LAS u32x4*)p; }
__device__ __forceinline__ u32x2 ldstr(lptr p) { return __builtin_bit_cast(u32x2, __builtin_amdgcn_ds_read_tr16_b64_v4i16((LAS s16x4*)p)); }
__device__ __forceinline__ u32x4 trfrag(lptr p, int o0, int o1) { u32x2 a = ldstr(p + o0), b = ldstr(p + o1); return u32x4{a.x, a.y, b.x, b.y}; }

__device__ __forceinline__ void poll(const char* ws, u32 off, u64 tag) {
  const GAS u64* p = (const GAS u64*)(ws + off);
  for (int s = 0; s < SPIN_CAP; ++s) {
    const u64 f = __hip_atomic_load(p, RLX_AGENT);
    if (__all(f == tag)) break;
  }
  __builtin_amdgcn_fence(__ATOMIC_ACQUIRE, "wavefront");
  asm volatile("" ::: "memory");
}
__device__ __forceinline__ void set_flag(char* ws, int ph, int id, u64 tag) { __hip_atomic_store((GAS u64*)(ws + WS_FLAG + (ph * 256 + id) * 128), tag, RLX_AGENT); }
__device__ __forceinline__ u32 flag_off(int ph, int id) { return WS_FLAG + (ph * 256 + id) * 128; }
__device__ __forceinline__ int kvslot(int rb, int j) {
  return ((rb >> 3) * 2 + (j >> 2)) * 32 + (rb & 7) * 4 + (j & 3);
}

#define PIN(x) asm volatile("" : "+v"(x))
template <int STRIDE> __device__ __forceinline__ void dma2(u32 lds, u32 voff, u32x4 rs, u32 soff) {
  asm volatile("s_mov_b32 m0, %1\n\ts_nop 0\n\t" DMA_L(0) DMA_A DMA_L(1024) : "+&s"(soff) : "s"(lds), "v"(voff), "s"(rs), "s"(STRIDE - 1024) : "memory", "scc");
}
__device__ __forceinline__ void dma1(u32 lds, u32 voff, u32x4 rs, u32 soff) {
  asm volatile("s_mov_b32 m0, %0\n\ts_nop 0\n\tbuffer_load_dwordx4 %1, %2, %3 offen lds" ::"s"(lds), "v"(voff), "s"(rs), "s"(soff) : "memory");
}
__device__ __forceinline__ f32x2 up2(u32 v) { return f32x2{lo16(v), hi16(v)}; }
__device__ __forceinline__ u32 pk2(f32x2 v) { return __builtin_bit_cast(u32, __builtin_convertvector(v, bf16x2_t)); }
__device__ __forceinline__ f32x2 fma2(f32x2 a, f32x2 b, f32x2 c) { return __builtin_elementwise_fma(a, b, c); }
__device__ __forceinline__ f32x2 bc2(float a) { return f32x2{a, a}; }
__device__ __forceinline__ void cnt_inc(int i) {
  asm volatile("s_waitcnt lgkmcnt(0)\n\ts_mov_b64 exec, 1\n\tds_add_u32 %0, %1\n\ts_mov_b64 exec, -1" ::"v"(L_CNT + 4 * i), "v"(1u) : "memory");
}
__device__ __forceinline__ u32 cnt_add(int i) {
  u32 old;
  asm volatile("s_waitcnt lgkmcnt(0)\n\ts_mov_b64 exec, 1\n\tds_add_rtn_u32 %0, %1, %2\n\ts_mov_b64 exec, -1\n\ts_waitcnt lgkmcnt(0)" : "=&v"(old) : "v"(L_CNT + 4 * i), "v"(1u) : "memory");
  return __builtin_amdgcn_readfirstlane(old);
}
__device__ __forceinline__ void cnt_wait(lptr L, int i, u32 n) {
  for (int s = 0; s < SPIN_CAP * 8; ++s)
    if ((u32)__builtin_amdgcn_readfirstlane(__hip_atomic_load((LAS u32*)(L + L_CNT) + i, RLX_WG)) >= n) break;
  asm volatile("" ::: "memory");
}

template <int NR> __device__ __forceinline__ void mod_partial(lptr L, const u32* cv, const u32x4* wm, int slot, int lane) {
  f32x2 a[4];
#pragma unroll
  for (int i = 0; i < NR; ++i) {
    const float cf = (i & 1) ? hi16(cv[i >> 1]) : lo16(cv[i >> 1]);
    float s = cf * __builtin_amdgcn_rcpf(1.f + __builtin_amdgcn_exp2f(-1.4426950408889634f * cf));
    s = lo16(pk(s, s));
#pragma unroll
    for (int e = 0; e < 4; ++e) a[e] = i == 0 ? bc2(s) * up2(wm[i][e]) : fma2(bc2(s), up2(wm[i][e]), a[e]);
  }
  const float b0 = add16(a[0].x, a[0].y), b1 = add16(a[1].x, a[1].y), b2 = add16(a[2].x, a[2].y), b3 = add16(a[3].x, a[3].y);
  float c0 = add32(b0, b1), c1 = add32(b2, b3);
  c0 += dpp<DPP_ROR8>(c0); c1 += dpp<DPP_ROR8>(c1);
  c0 += dpp<DPP_ROR4>(c0); c1 += dpp<DPP_ROR4>(c1);
  if ((lane & 12) == 0) {
    LAS float* p = (LAS float*)(L + L_PART) + slot * 32 + 8 * (lane & 3) + (lane >> 4);
    p[0] = c0; p[4] = c1;
  }
}
template <int NP> __device__ __forceinline__ void mod_publish(lptr L, char* ws, int strip, int lane, u64 tag) {
  if (lane < 32) {
    LAS float* p = (LAS float*)(L + L_PART) + lane;
    float s = (p[0] + p[32]) + (p[64] + p[96]);
    if (NP == 8) s += (p[128] + p[160]) + (p[192] + p[224]);
    const u64 g = (tag << 16) | (u64)(pk(s, s) & 0xffffu);
    __hip_atomic_store((GAS u64*)(ws + WS_MODG + (strip * 32 + lane) * 8), g, RLX_AGENT);
  }
}
__device__ __forceinline__ bool gran_ok(u32x4 g, u64 tag) {
  const u32 t0 = (u32)tag << 16, t1 = (u32)(tag >> 16);
  return (((g.x ^ t0) | (g.z ^ t0)) >> 16 == 0) && g.y == t1 && g.w == t1;
}
__device__ __forceinline__ u32 gran_val(u32x4 g, bool plus1) {
  if (plus1) return pk(1.f + lo16(g.x), 1.f + lo16(g.z));
  return (g.x & 0xffffu) | (g.z << 16);
}

#define MARK(i) do {} while (0)

__global__ void __launch_bounds__(512, 2)
fused_block(const u16* __restrict__ x, const u16* __restrict__ wqkv, const u16* __restrict__ wo, const u16* __restrict__ w1, const u16* __restrict__ b1,
            const u16* __restrict__ w2, const u16* __restrict__ b2, const u16* __restrict__ c, const u16* __restrict__ wmod, u16* __restrict__ out, char* ws) {
  __shared__ __attribute__((aligned(16))) char smem[LDS_BYTES];
  const lptr L = (lptr)smem;
  MARK(0);
  const int b = blockIdx.x, xcd = b & 7, bi = b >> 3;
  const int t = threadIdx.x, lane = t & 63, w = __builtin_amdgcn_readfirstlane(t >> 6);
  const int rbA = xcd * 4 + (bi >> 3), jA = bi & 7;
  const int rbB = (xcd >> 1) * 8 + (bi >> 2), jB = (xcd & 1) * 4 + (bi & 3);
  const int strip = 2 * (b & 7) + ((b >> 3) & 1) + 16 * (b >> 4);
  const u32x4 rsW = mkrs(ws, WS_END);
  const __amdgpu_buffer_rsrc_t rW = __builtin_amdgcn_make_buffer_rsrc(ws, 0, WS_END, 0x00020000);

  const u32x4 rsQ = mkrs(wqkv, 512u * 3072u), rsM = mkrs(wmod, 512u * 6144u), rsX = mkrs(x, 2048u * 1024u);
  if (w == 0) {
    u64 pc;
    u32 len;
    asm volatile("s_getpc_b64 %0\n.Lpf_here:\n\ts_mov_b32 %1, .Lpf_end-.Lpf_here" : "=s"(pc), "=s"(len));
    touch((u32)lane * 128, mkrs((const void*)pc, len), (u32)(bi & 3) * 8192);
  }
  if (w == 1 && bi < 16) touch((u32)lane * 6144, rsM, (u32)(64 * (bi >> 1)) * 6144 + (xcd + 8 * (bi & 1)) * 128);
  if (w == 2) touch((u32)lane * 128, rsX, (u32)(rbB * 64 + jB * 8) * 1024);
  if (w == 3) touch(lane < 48 ? (u32)(lane & 15) * 3072 + (lane >> 4) * 1024 : 0x7fffffffu, rsQ, (u32)(16 * rbB) * 3072 + jB * 128);
  u32x4 cv, wm[8], xr[8];
  const u32 mvoff = (u32)((t & 255) >> 2) * (8 * 6144) + strip * 64 + (t & 3) * 16;
  {
    const u32x4 rsC = mkrs(c, 1024u);
    u32 so = 0;
    asm volatile("s_cmp_lt_u32 %14, 32\n\ts_cbranch_scc0 .Lnomod\n\t"
                 "s_cmp_lt_u32 %15, 4\n\ts_cbranch_scc1 .Lmodbar\n\t"
                 "buffer_load_dwordx4 %8, %12, %13, 0 offen\n\t"
                 "buffer_load_dwordx4 %0, %10, %11, %9 offen\n\ts_addk_i32 %9, 0x1800\n\t"
                 "buffer_load_dwordx4 %1, %10, %11, %9 offen\n\ts_addk_i32 %9, 0x1800\n\t"
                 "buffer_load_dwordx4 %2, %10, %11, %9 offen\n\ts_addk_i32 %9, 0x1800\n\t"
                 "buffer_load_dwordx4 %3, %10, %11, %9 offen\n\ts_addk_i32 %9, 0x1800\n\t"
                 "buffer_load_dwordx4 %4, %10, %11, %9 offen\n\ts_addk_i32 %9, 0x1800\n\t"
                 "buffer_load_dwordx4 %5, %10, %11, %9 offen\n\ts_addk_i32 %9, 0x1800\n\t"
                 "buffer_load_dwordx4 %6, %10, %11, %9 offen\n\ts_addk_i32 %9, 0x1800\n\t"
                 "buffer_load_dwordx4 %7, %10, %11, %9 offen\n"
                 ".Lmodbar:\n\ts_barrier\n.Lnomod:"
                 : "=&v"(wm[0]), "=&v"(wm[1]), "=&v"(wm[2]), "=&v"(wm[3]), "=&v"(wm[4]), "=&v"(wm[5]), "=&v"(wm[6]), "=&v"(wm[7]), "=&v"(cv), "+&s"(so)
                 : "v"(mvoff), "s"(rsM), "v"((u32)((t & 255) >> 2) * 16), "s"(rsC), "s"(b), "s"(w)
                 : "memory", "scc");
  }
  {
    const u32 so = (u32)(rbB * 64 + w * 8) * 1024;
    asm volatile("buffer_load_dwordx4 %0, %8, %9, %10 offen\n\t"
                 "buffer_load_dwordx4 %1, %8, %9, %10 offen offset:1024\n\t"
                 "buffer_load_dwordx4 %2, %8, %9, %10 offen offset:2048\n\t"
                 "buffer_load_dwordx4 %3, %8, %9, %10 offen offset:3072\n\t"
                 "buffer_load_dwordx4 %4, %8, %9, %11 offen\n\t"
                 "buffer_load_dwordx4 %5, %8, %9, %11 offen offset:1024\n\t"
                 "buffer_load_dwordx4 %6, %8, %9, %11 offen offset:2048\n\t"
                 "buffer_load_dwordx4 %7, %8, %9, %11 offen offset:3072"
                 : "=&v"(xr[0]), "=&v"(xr[1]), "=&v"(xr[2]), "=&v"(xr[3]), "=&v"(xr[4]), "=&v"(xr[5]), "=&v"(xr[6]), "=&v"(xr[7])
                 : "v"((u32)lane * 16), "s"(rsX), "s"(so), "s"(so + 4096)
                 : "memory");
  }
  if (t < 20) *(LAS u32*)(L + L_ONES + 4 * t) = t < 4 ? 0x3f803f80u : 0u;
  BAR();
  u64 tag = *(const __attribute__((address_space(4))) u64*)(ws + WS_EPOCH);
  MARK(1);
  if (b < 32 && w >= 4) {
    asm volatile("s_waitcnt vmcnt(8)" : "+v"(wm[0]), "+v"(wm[1]), "+v"(wm[2]), "+v"(wm[3]), "+v"(wm[4]), "+v"(wm[5]), "+v"(wm[6]), "+v"(wm[7]), "+v"(cv)::"memory");
    const u32 cw[4] = {cv.x, cv.y, cv.z, cv.w};
    mod_partial<8>(L, cw, wm, w - 4, lane);
    if (cnt_add(C_MOD) == 3) mod_publish<4>(L, ws, strip, lane, tag + 1);
  }
  MARK(2);
  const u32 wvoff = (u32)(lane >> 3) * 3072 + jB * 128 + (u32)((lane & 7) ^ (((lane >> 4) & 1) << 2)) * 16;
  dma16<8 * 3072>(16384 * w, wvoff, rsQ, (w < 4 ? 1024 : 2048) + 128 * (w & 3) * 3072);
  asm volatile("s_waitcnt vmcnt(16)" : "+v"(xr[0]), "+v"(xr[1]), "+v"(xr[2]), "+v"(xr[3]), "+v"(xr[4]), "+v"(xr[5]), "+v"(xr[6]), "+v"(xr[7])::"memory");
  tag += 1;
  asm volatile("" : "+s"(tag));
  MARK(3);
  f32x2 xn[8][4];
  {
    float s[8];
#pragma unroll
    for (int r = 0; r < 8; ++r) {
#pragma unroll
      for (int e = 0; e < 4; ++e) xn[r][e] = up2(xr[r][e]);
      const f32x2 a = (xn[r][0] + xn[r][1]) + (xn[r][2] + xn[r][3]);
      s[r] = a.x + a.y;
    }
    auto red8 = [&](float (&v)[8], float& c0, float& c1) {
      const float b0 = add16(v[0], v[1]), b1 = add16(v[2], v[3]), b2 = add16(v[4], v[5]), b3 = add16(v[6], v[7]);
      c0 = add32(b0, b1); c1 = add32(b2, b3);
      c0 += dpp<DPP_X1>(c0); c1 += dpp<DPP_X1>(c1); c0 += dpp<DPP_X2>(c0); c1 += dpp<DPP_X2>(c1);
      c0 += dpp<DPP_ROR4>(c0); c1 += dpp<DPP_ROR4>(c1); c0 += dpp<DPP_ROR8>(c0); c1 += dpp<DPP_ROR8>(c1);
    };
    float c0, c1;
    red8(s, c0, c1);
    c0 *= (-1.f / 512.f); c1 *= (-1.f / 512.f);
#pragma unroll
    for (int r = 0; r < 8; ++r) {
      const float m = asf(__builtin_amdgcn_readlane(asu(r < 4 ? c0 : c1), (r & 3) * 16));
#pragma unroll
      for (int e = 0; e < 4; ++e) xn[r][e] += bc2(m);
      const f32x2 q = fma2(xn[r][3], xn[r][3], fma2(xn[r][2], xn[r][2], fma2(xn[r][1], xn[r][1], xn[r][0] * xn[r][0])));
      s[r] = q.x + q.y;
    }
    red8(s, c0, c1);
    c0 = __builtin_amdgcn_rsqf(__builtin_fmaf(c0, 1.f / 512.f, 1e-5f)); c1 = __builtin_amdgcn_rsqf(__builtin_fmaf(c1, 1.f / 512.f, 1e-5f));
#pragma unroll
    for (int r = 0; r < 8; ++r) {
      const float rs = asf(__builtin_amdgcn_readlane(asu(r < 4 ? c0 : c1), (r & 3) * 16));
#pragma unroll
      for (int e = 0; e < 4; ++e) { xn[r][e] *= bc2(rs); PIN(xn[r][e]); }
    }
  }
  MARK(4);
  VMCNT(0);
  cnt_inc(w < 4 ? C_WK : C_WV);
  const int pr = w & 1, th = (w >> 1) & 1;
  const int G = lane >> 4, q4 = (lane >> 2) & 3, pp = lane & 3;
  const u32 wtr = (u32)(8 * (G >> 1) + q4) * 128 + (u32)((64 * pr) ^ (64 * ((q4 >> 1) & 1))) + 32 * (G & 1) + 8 * pp;
  u32x4 wf[32];
  {
    cnt_wait(L, w < 4 ? C_WK : C_WV, 4);
    MARK(5);
    const lptr p = L + (w < 4 ? 0u : L_STG) + wtr;
#pragma unroll
    for (int ks = 0; ks < 32; ++ks) wf[ks] = trfrag(p, ks * 2048, ks * 2048 + 512);
    SCHED();
    MARK(6);
    if (w < 4) {
      for (int s = 0; s < SPIN_CAP; ++s) {
        asm volatile("" ::: "memory");
        const u32x4 g0 = __builtin_amdgcn_raw_buffer_load_b128(rW, WS_MODG + t * 16, 0, 16);
        const u32x4 g1 = __builtin_amdgcn_raw_buffer_load_b128(rW, WS_MODG + 4096 + t * 16, 0, 16);
        *(LAS u32*)(L + L_MOD + 4 * t) = gran_val(g0, false);
        *(LAS u32*)(L + L_MOD + 1024 + 4 * t) = gran_val(g1, true);
        if (__all(gran_ok(g0, tag) && gran_ok(g1, tag))) break;
      }
    }
  }
  BAR();
  MARK(7);
  u32 rt_dst, rt_step;
  u32 rt_rd, rt_u;
  if (w < 4) {
    const u16* src; u32 rowb, so;
    if (b < 112) {
      const int lc = b >> 2, ks = 4 * (b & 3) + w;
      src = w1; rowb = 3584; so = 32 * ks * 3584 + lc * 128;
      rt_dst = WS_W1T + (2 * lc * 16 + ks) * 2048; rt_step = 32768; rt_rd = (8 * G + q4) * 128; rt_u = 4 * 128;
    } else if (b < 144) {
      const int j = (b - 112) >> 2, ks = 4 * (b & 3) + w;
      src = wo; rowb = 1024; so = 32 * ks * 1024 + j * 128;
      rt_dst = WS_WOT + (j * 16 + ks) * 4096; rt_step = 2048; rt_rd = (16 * (G >> 1) + 4 * (G & 1) + q4) * 128; rt_u = 8 * 128;
    } else {
      const int j = (b - 144) / 14, ks = 4 * ((b - 144) % 14) + w;
      src = w2; rowb = 1024; so = 32 * ks * 1024 + j * 128;
      rt_dst = WS_W2T + (j * 56 + ks) * 4096; rt_step = 2048; rt_rd = (4 * G + q4) * 128; rt_u = 16 * 128;
    }
    const u32x4 rs = mkrs(src, 1792u * 1024u);
    const u32 vo = (u32)(lane >> 3) * rowb + (lane & 7) * 16;
#pragma unroll
    for (int q = 0; q < 4; ++q) dma1(L_RT + w * 4096 + q * 1024, vo, rs, so + q * 8 * rowb);
  } else {
    rt_dst = rt_step = rt_rd = rt_u = 0;
    if (w == 4) touch((u32)(lane & 7) * 32768 + b * 128, rsW, WS_FLAG);
    if (w == 5) touch((u32)lane * 6144, rsM, (u32)(64 * (bi >> 2)) * 6144 + (16 + xcd + 8 * (bi & 3)) * 128);
  }
  {
    const u32x4 sa = lds128(L + L_MOD + lane * 16), ga = lds128(L + L_MOD + 1024 + lane * 16);
    const lptr hp = L + L_H + (w >> 2) * 32768 + lane * 512 + (8 * (w & 3)) * 16;
    f32x2 sf[4], gf[4];
#pragma unroll
    for (int e = 0; e < 4; ++e) { sf[e] = up2(sa[e]); gf[e] = up2(ga[e]); }
#pragma unroll
    for (int r = 0; r < 8; ++r) {
      u32x4 o;
#pragma unroll
      for (int e = 0; e < 4; ++e) o[e] = pk2(fma2(xn[r][e], gf[e], sf[e]));
      *(LAS u32x4*)(hp + (r ^ (lane & 7)) * 16) = o;
      SCHED();
    }
  }
  BAR();
  MARK(8);

  const int r32 = lane & 31, hh = lane >> 5;
  const int slotB = kvslot(rbB, jB), idB = rbB * 8 + jB, idA = rbA * 8 + jA;
  const bool late = b >= 32 && b < 96;
  lptr hb[4];
#pragma unroll
  for (int i = 0; i < 4; ++i) hb[i] = L + L_H + th * 32768 + hh * 512 + (r32 ^ ((2 * i + hh) & 7)) * 16;
  constexpr int PD = 6;
  u32x4 o0, o1, q0, q1;
  u32 lo;
  if (w < 4) {
    __builtin_amdgcn_s_setprio(2);
    f32x16 acc = {};
    {
      u32x4 f[32];
#pragma unroll
      for (int i = 0; i < PD; ++i) f[i] = lds128(hb[i & 3] + i * 1024);
      SCHED();
#pragma unroll
      for (int ks = 0; ks < 32; ++ks) {
        if (ks + PD < 32) f[ks + PD] = lds128(hb[(ks + PD) & 3] + (ks + PD) * 1024);
        acc = MFMA32(wf[ks], f[ks], acc);
        SCHED();
      }
    }
#pragma unroll
    for (int e = 0; e < 4; ++e) { o0[e] = pk(acc[2 * e], acc[2 * e + 1]); o1[e] = pk(acc[8 + 2 * e], acc[8 + 2 * e + 1]); }
    lo = (th * 4 + 2 * pr) * 1024 + lane * 16;
    __builtin_amdgcn_raw_buffer_store_b128(o0, rW, lo + WS_K + slotB * 8192, 0, 16);
    __builtin_amdgcn_raw_buffer_store_b128(o1, rW, lo + 1024 + WS_K + slotB * 8192, 0, 16);
    __builtin_amdgcn_s_setprio(0);
    MARK(9);
    VMCNT(2);
#pragma unroll
    for (int f = 0; f < 4; ++f) {
      const lptr p = L + L_RT + w * 4096 + rt_rd + 32 * f + 8 * pp;
      const u32x2 a = ldstr(p), bq = ldstr(p + rt_u);
      __builtin_amdgcn_raw_buffer_store_b128(u32x4{a.x, a.y, bq.x, bq.y}, rW, rt_dst + (f >> 1) * rt_step + (f & 1) * 1024 + lane * 16, 0, 16);
    }
    VMCNT(4);
    if (cnt_add(C_K) == 3 && lane == 0) set_flag(ws, F_K, idB, tag);
    MARK(10);
    cnt_wait(L, C_WQ, 4);
    MARK(11);
    acc = f32x16{};
    {
      const lptr p = L + L_STG + wtr;
      u32x4 f[32], a[32];
      constexpr int QD = 5;
#pragma unroll
      for (int i = 0; i < QD; ++i) { a[i] = trfrag(p, i * 2048, i * 2048 + 512); f[i] = lds128(hb[i & 3] + i * 1024); }
      SCHED();
#pragma unroll
      for (int ks = 0; ks < 32; ++ks) {
        acc = MFMA32(a[ks], f[ks], acc);
        if (ks + QD < 32) { a[ks + QD] = trfrag(p, (ks + QD) * 2048, (ks + QD) * 2048 + 512); f[ks + QD] = lds128(hb[(ks + QD) & 3] + (ks + QD) * 1024); }
        SCHED();
      }
    }
#pragma unroll
    for (int e = 0; e < 4; ++e) { q0[e] = pk(acc[2 * e] * QSCALE, acc[2 * e + 1] * QSCALE); q1[e] = pk(acc[8 + 2 * e] * QSCALE, acc[8 + 2 * e + 1] * QSCALE); }
    PIN(q0); PIN(q1);
  } else {
    dma16<8 * 3072>(L_STG + 16384 * (w - 4), wvoff, rsQ, 128 * (w - 4) * 3072);
    __builtin_amdgcn_s_setprio(1);
    const __amdgpu_buffer_rsrc_t rM = __builtin_amdgcn_make_buffer_rsrc((void*)wmod, 0, late ? 512 * 6144 : 0, 0x00020000);
    const __amdgpu_buffer_rsrc_t rC = __builtin_amdgcn_make_buffer_rsrc((void*)c, 0, 1024, 0x00020000);
    u32x4 lm[8], lc;
    f32x16 acc = {};
    {
      u32x4 f[32];
#pragma unroll
      for (int i = 0; i < PD; ++i) f[i] = lds128(hb[i & 3] + i * 1024);
      SCHED();
#pragma unroll
      for (int ks = 0; ks < 32; ++ks) {
        if (ks + PD < 32) f[ks + PD] = lds128(hb[(ks + PD) & 3] + (ks + PD) * 1024);
        acc = MFMA32(f[ks], wf[ks], acc);
        if (ks == 1) lc = __builtin_amdgcn_raw_buffer_load_b128(rC, ((t - 256) >> 2) * 16, 0, 0);
        if (ks >= 3 && ks < 27 && ks % 3 == 0) lm[ks / 3 - 1] = __builtin_amdgcn_raw_buffer_load_b128(rM, mvoff, (ks / 3 - 1) * 6144, 0);
        if (ks == 28) { VMCNT(9); cnt_inc(C_WQ); }
        SCHED();
      }
    }
#pragma unroll
    for (int e = 0; e < 4; ++e) { o0[e] = pk(acc[2 * e], acc[2 * e + 1]); o1[e] = pk(acc[8 + 2 * e], acc[8 + 2 * e + 1]); }
    lo = (th * 4 + 2 * pr + (r32 >> 4)) * 1024 + hh * 256 + (lane & 15) * 16;
    __builtin_amdgcn_raw_buffer_store_b128(o0, rW, lo + WS_V + slotB * 8192, 0, 16);
    __builtin_amdgcn_raw_buffer_store_b128(o1, rW, lo + 512 + WS_V + slotB * 8192, 0, 16);
    __builtin_amdgcn_s_setprio(0);
    SCHED();
    MARK(9);
    if (late) {
      const u32 cw[4] = {lc.x, lc.y, lc.z, lc.w};
      mod_partial<8>(L, cw, lm, w - 4, lane);
    }
    q0 = q1 = u32x4{};
    MARK(10);
  }
  VMCNT(0);
  BAR();
  MARK(12);

  const int mt = w >> 1, kh = w & 1, tok = lane & 15;
  const int col0 = 64 * jA + 32 * kh + 16 * (G & 1) + 8 * (G >> 1);
  u32x4 w1f[2][16];
#define W1_LD(ks) do { w1f[0][ks] = __builtin_amdgcn_raw_buffer_load_b128(rW, lane * 16 + ((ks) & 1) * 2048, WS_W1T + (7 * jB + w) * 32768 + ((ks) >> 1) * 4096, 16); \
                       w1f[1][ks] = __builtin_amdgcn_raw_buffer_load_b128(rW, lane * 16 + ((ks) & 1) * 2048 + 1024, WS_W1T + (7 * jB + w) * 32768 + ((ks) >> 1) * 4096, 16); } while (0)
  {
    const int prb = rbB - 4 + (w & 3);
    if (w < 4) {
      *(LAS u32x4*)(L + L_KIMG + 32768 + lo) = o0; *(LAS u32x4*)(L + L_KIMG + 32768 + lo + 1024) = o1;
      *(LAS u32x4*)(L + L_Q + lo) = q0; *(LAS u32x4*)(L + L_Q + lo + 1024) = q1;
      cnt_inc(C_OWN);
    } else {
      if (w == 4 && lane < 2) __hip_atomic_store((GAS u64*)(ws + (lane ? flag_off(F_W, b) : flag_off(F_V, idB))), tag, RLX_AGENT);
      if (w == 5 && late) mod_publish<4>(L, ws, strip, lane, tag);
      *(LAS u32x4*)(L + L_VIMG + 32768 + lo) = o0; *(LAS u32x4*)(L + L_VIMG + 32768 + lo + 512) = o1;
    }
    if (prb >= 0) {
      poll(ws, flag_off(w < 4 ? F_K : F_V, prb * 8 + jB), tag);
      MARK(13);
      const u32 so = (w < 4 ? WS_K : WS_V) + kvslot(prb, jB) * 8192, ld = (w < 4 ? L_KIMG : L_VIMG) + (w & 3) * 8192;
      dma4_sc1(ld, lane * 16, rsW, so);
      dma4_sc1(ld + 4096, lane * 16, rsW, so + 4096);
    }
    if (w >= 4) {
      VMCNT(0);
      cnt_inc(C_VH);
      if (w == 7) {
        const int lw = lane < 34 ? lane : 0;
        poll(ws, flag_off(F_W, lw < 16 ? 4 * ((7 * jB) >> 1) + lw : lw < 20 ? 112 + 4 * jA + (lw - 16) : 144 + 14 * jA + (lw - 20)), tag);
        cnt_inc(C_WF);
      }
      cnt_wait(L, C_WF, 1);
      MARK(14);
#pragma unroll
      for (int q = 0; q < 4; ++q) dma4_sc1(L_WO + (w - 4) * 16384 + q * 4096, lane * 16, rsW, WS_WOT + jA * 65536 + (w - 4) * 16384 + q * 4096);
    }
  }
  {
    const int head = w & 3, qh = w >> 2;
    const lptr kp = L + L_KIMG + (qh * 4 + head) * 1024 + lane * 16;
    const f32x16 zero = {};
    const bool vl = r32 < 16;
    const u32 vbase = vl ? L_VIMG + (qh * 4 + head) * 1024 + hh * 256 + r32 * 16 : L_ONES, vts = vl ? 4096u : 0u, vss = vl ? 512u : 0u;
#define EXP2(v) __builtin_amdgcn_exp2f(v)
#define PK4(s, o) u32x4{pk(EXP2(s[o]), EXP2(s[o + 1])), pk(EXP2(s[o + 2]), EXP2(s[o + 3])), pk(EXP2(s[o + 4]), EXP2(s[o + 5])), pk(EXP2(s[o + 6]), EXP2(s[o + 7]))}
#define CAUSAL(v) (((v) & 3) + 8 * ((v) >> 2) + 4 * hh <= r32)
#define VRD(A, B, i) do { A = lds128(L + vbase + (i) * vts); B = lds128(L + vbase + (i) * vts + vss); } while (0)
    cnt_wait(L, C_OWN, 4);
    const u32x4 qf = lds128(L + L_Q + (qh * 4 + head) * 1024 + lane * 16);
    f32x16 acc;
    bool exact = rbB < 4;
    if (!exact) {
      f32x16 sA = MFMA32(lds128(kp + 8 * 4096), qf, zero), sB;
#pragma unroll
      for (int v = 0; v < 16; ++v) sA[v] = CAUSAL(v) ? sA[v] : -INFINITY;
      float m = sA[0];
#pragma unroll
      for (int v = 1; v < 16; ++v) m = __builtin_fmaxf(m, sA[v]);
      { auto r = __builtin_amdgcn_permlane32_swap(asu(m), asu(m), false, false); m = __builtin_fmaxf(asf(r[0]), asf(r[1])); }
      f32x16 nm;
#pragma unroll
      for (int v = 0; v < 16; ++v) { sA[v] -= m; nm[v] = -m; sB[v] = CAUSAL(v) ? -INFINITY : -m; }
      u32x4 p80 = PK4(sA, 0), p81 = PK4(sA, 8);
      PIN(p80); PIN(p81); PIN(nm); PIN(sB);
      if (w < 4) { VMCNT(0); cnt_inc(C_KH); __builtin_amdgcn_s_setprio(2); }
      cnt_wait(L, C_KH, 4);
      MARK(15);
      u32x4 kf[8];
#pragma unroll
      for (int i = 0; i < 8; ++i) kf[i] = lds128(kp + i * 4096);
      SCHED();
      sB = MFMA32(kf[0], qf, sB);
      sA = MFMA32(kf[1], qf, nm);
      SCHED();
      u32x4 pa0 = PK4(sB, 0), pa1 = PK4(sB, 8);
      PIN(pa0); PIN(pa1);
      sB = MFMA32(kf[2], qf, nm);
      SCHED();
      u32x4 pb0 = PK4(sA, 0), pb1 = PK4(sA, 8);
      PIN(pb0); PIN(pb1);
      sA = MFMA32(kf[3], qf, nm);
      SCHED();
      u32x4 pc0 = PK4(sB, 0), pc1 = PK4(sB, 8);
      PIN(pc0); PIN(pc1);
      sB = MFMA32(kf[4], qf, nm);
      SCHED();
      cnt_wait(L, C_VH, 4);
      MARK(16);
      u32x4 v0, v1, v2, v3;
      VRD(v0, v1, 8); VRD(v2, v3, 0);
      SCHED();
      u32x4 pd0 = PK4(sA, 0);
      PIN(pd0);
      acc = MFMA32(v0, p80, zero); acc = MFMA32(v1, p81, acc);
      VRD(v0, v1, 1);
      SCHED();
      u32x4 pd1 = PK4(sA, 8);
      PIN(pd1);
      sA = MFMA32(kf[5], qf, nm);
      acc = MFMA32(v2, pa0, acc); acc = MFMA32(v3, pa1, acc);
      VRD(v2, v3, 2);
      SCHED();
      pa0 = PK4(sB, 0);
      PIN(pa0);
      acc = MFMA32(v0, pb0, acc); acc = MFMA32(v1, pb1, acc);
      VRD(v0, v1, 3);
      SCHED();
      pa1 = PK4(sB, 8);
      PIN(pa1);
      sB = MFMA32(kf[6], qf, nm);
      acc = MFMA32(v2, pc0, acc); acc = MFMA32(v3, pc1, acc);
      VRD(v2, v3, 4);
      SCHED();
      pb0 = PK4(sA, 0);
      PIN(pb0);
      acc = MFMA32(v0, pd0, acc); acc = MFMA32(v1, pd1, acc);
      VRD(v0, v1, 5);
      SCHED();
      pb1 = PK4(sA, 8);
      PIN(pb1);
      sA = MFMA32(kf[7], qf, nm);
      acc = MFMA32(v2, pa0, acc); acc = MFMA32(v3, pa1, acc);
      VRD(v2, v3, 6);
      SCHED();
      pc0 = PK4(sB, 0);
      PIN(pc0);
      acc = MFMA32(v0, pb0, acc); acc = MFMA32(v1, pb1, acc);
      VRD(v0, v1, 7);
      SCHED();
      pc1 = PK4(sB, 8);
      PIN(pc1);
      acc = MFMA32(v2, pc0, acc);
      SCHED();
      pd0 = PK4(sA, 0);
      PIN(pd0);
      acc = MFMA32(v3, pc1, acc); acc = MFMA32(v0, pd0, acc);
      SCHED();
      pd1 = PK4(sA, 8);
      acc = MFMA32(v1, pd1, acc);
      exact = __any(!(acc[8] < 1.0995116e12f));
    } else {
      if (w < 4) { VMCNT(0); cnt_inc(C_KH); }
      cnt_wait(L, C_KH, 4);
      cnt_wait(L, C_VH, 4);
    }
    if (exact) {
      const int istart = 8 - 2 * rbB - qh;
      const int i1 = istart > 1 ? istart : 1;
      f32x16 s8 = MFMA32(lds128(kp + 8 * 4096), qf, zero), s0;
#pragma unroll
      for (int v = 0; v < 16; ++v) s0[v] = -INFINITY;
      if (istart <= 0) s0 = MFMA32(lds128(kp), qf, zero);
#pragma unroll
      for (int v = 0; v < 16; ++v) { s8[v] = CAUSAL(v) ? s8[v] : -INFINITY; s0[v] = CAUSAL(v) ? -INFINITY : s0[v]; }
      float m = -INFINITY;
#pragma unroll
      for (int v = 0; v < 16; ++v) m = __builtin_fmaxf(__builtin_fmaxf(m, s8[v]), s0[v]);
#pragma clang loop unroll(disable)
      for (int i = i1; i < 8; ++i) {
        const f32x16 s = MFMA32(lds128(kp + i * 4096), qf, zero);
#pragma unroll
        for (int v = 0; v < 16; ++v) m = __builtin_fmaxf(m, s[v]);
      }
      { auto r = __builtin_amdgcn_permlane32_swap(asu(m), asu(m), false, false); m = __builtin_fmaxf(asf(r[0]), asf(r[1])); }
      f32x16 nm;
#pragma unroll
      for (int v = 0; v < 16; ++v) { s8[v] -= m; s0[v] -= m; nm[v] = -m; }
      acc = MFMA32(lds128(L + vbase + 8 * vts), PK4(s8, 0), zero);
      acc = MFMA32(lds128(L + vbase + 8 * vts + vss), PK4(s8, 8), acc);
      if (istart <= 0) {
        acc = MFMA32(lds128(L + vbase), PK4(s0, 0), acc);
        acc = MFMA32(lds128(L + vbase + vss), PK4(s0, 8), acc);
      }
#pragma clang loop unroll(disable)
      for (int i = i1; i < 8; ++i) {
        const f32x16 s = MFMA32(lds128(kp + i * 4096), qf, nm);
        acc = MFMA32(lds128(L + vbase + i * vts), PK4(s, 0), acc);
        acc = MFMA32(lds128(L + vbase + i * vts + vss), PK4(s, 8), acc);
      }
    }
    const float rl = __builtin_amdgcn_rcpf(acc[8]);
    u32x4 o;
#pragma unroll
    for (int e = 0; e < 4; ++e) o[e] = pk(acc[2 * e] * rl, acc[2 * e + 1] * rl);
    const u32 go = ((2 * qh + (r32 >> 4)) * 16 + (head >> 1)) * 1024 + ((2 * (head & 1) + hh) * 16 + (lane & 15)) * 16;
    __builtin_amdgcn_raw_buffer_store_b128(o, rW, go + WS_O + (rbB * 64 + 2 * jB) * 1024, 0, 16);
    __builtin_amdgcn_s_setprio(0);
  }
  MARK(17);
  if (w < 4) {
    for (int s = 0; s < SPIN_CAP; ++s) {
      asm volatile("" ::: "memory");
      bool ok = true;
#pragma unroll
      for (int k = 0; k < 4; ++k) {
        const u32x4 g = __builtin_amdgcn_raw_buffer_load_b128(rW, WS_MODG + 8192 + k * 4096 + t * 16, 0, 16);
        *(LAS u32*)(L + L_MOD + 2048 + k * 1024 + 4 * t) = gran_val(g, k == 2);
        ok = ok && gran_ok(g, tag);
      }
      if (__all(ok)) break;
    }
  }
  VMCNT(0);
  if (cnt_add(C_O) == 7 && lane == 0) set_flag(ws, F_O, idB, tag);
  cnt_wait(L, C_WF, 1);
  MARK(18);
  const u32x4 xres = *(const GAS u32x4*)(x + (size_t)(rbA * 64 + mt * 16 + tok) * 512 + col0);
  if (w < 7) {
#pragma unroll
    for (int ks = 0; ks < 16; ++ks) W1_LD(ks);
  }
  SCHED();
  cnt_wait(L, C_O, 8);
  MARK(19);

  u32x4 x1p;
  {
    poll(ws, flag_off(F_O, rbA * 8 + 4 * kh + (lane & 3)), tag);
    MARK(20);
    u32x4 of[8];
#pragma unroll
    for (int k = 0; k < 8; ++k) of[k] = __builtin_amdgcn_raw_buffer_load_b128(rW, (k & 3) * 1024 + lane * 16, WS_O + ((rbA * 4 + mt) * 16 + 8 * kh + (k & 4)) * 1024, 16);
    SCHED();
    const lptr wb = L + L_WO + kh * 32768 + lane * 16;
    u32x4 a[8][4];
    constexpr int OD = 3;
#pragma unroll
    for (int k = 0; k < OD; ++k)
#pragma unroll
      for (int nt = 0; nt < 4; ++nt) a[k][nt] = lds128(wb + k * 4096 + nt * 1024);
    SCHED();
    f32x4 acc[4] = {};
#pragma unroll
    for (int k = 0; k < 8; ++k) {
#pragma unroll
      for (int nt = 0; nt < 4; ++nt) {
        acc[nt] = MFMA16(a[k][nt], of[k], acc[nt]);
        if (k + OD < 8) a[k + OD][nt] = lds128(wb + (k + OD) * 4096 + nt * 1024);
        SCHED();
      }
    }
    *(LAS f32x4*)(L + L_XCH + (w * 2 + 0) * 1024 + lane * 16) = kh ? acc[0] : acc[2];
    *(LAS f32x4*)(L + L_XCH + (w * 2 + 1) * 1024 + lane * 16) = kh ? acc[1] : acc[3];
    const u32x4 ga = lds128(L + L_MOD + 2048 + col0 * 2), g2 = lds128(L + L_MOD + 4096 + col0 * 2);
    BAR();
    MARK(21);
    f32x4 t0 = (kh ? acc[2] : acc[0]) + *(LAS f32x4*)(L + L_XCH + ((w ^ 1) * 2 + 0) * 1024 + lane * 16);
    f32x4 t1 = (kh ? acc[3] : acc[1]) + *(LAS f32x4*)(L + L_XCH + ((w ^ 1) * 2 + 1) * 1024 + lane * 16);
    float y[8];
#pragma unroll
    for (int r = 0; r < 4; ++r) { auto s = __builtin_amdgcn_permlane16_swap(asu(t0[r]), asu(t1[r]), false, false); y[r] = asf(s[0]); y[4 + r] = asf(s[1]); }
    u32x4 xg;
    f32x2 xv[4];
#pragma unroll
    for (int e = 0; e < 4; ++e) {
      x1p[e] = pk2(fma2(up2(ga[e]), f32x2{y[2 * e], y[2 * e + 1]}, up2(xres[e])));
      xv[e] = up2(x1p[e]);
      xg[e] = pk2(xv[e] * up2(g2[e]));
    }
    __builtin_amdgcn_raw_buffer_store_b128(xg, rW, ((2 * (G & 1) + (G >> 1)) * 16 + tok) * 16 + WS_X + ((rbA * 4 + mt) * 16 + 2 * jA + kh) * 1024, 0, 16);
    const f32x2 s1 = (xv[0] + xv[1]) + (xv[2] + xv[3]), s2 = fma2(xv[3], xv[3], fma2(xv[2], xv[2], fma2(xv[1], xv[1], xv[0] * xv[0])));
    float z = add16(s1.x + s1.y, s2.x + s2.y);
    z = add32(z, z);
    if (lane < 32) __builtin_amdgcn_raw_buffer_store_b32(asu(z), rW, lane * 4 + WS_ST + ((rbA * 4 + mt) * 16 + 2 * jA + kh) * 128, 0, 16);
  }
  SCHED();
  const u32 w2s = WS_W2T + (jA * 56 + 8 * w) * 4096, sl2 = L_W2 + w * 8192;
  const u32 sl2x = w < 4 ? w * 4096 : w < 6 ? 81920 + (w - 4) * 4096 : 147456;
  u32x4 w2f[8][4];
#define W2_LD(k, nt) w2f[k][nt] = __builtin_amdgcn_raw_buffer_load_b128(rW, lane * 16 + (nt) * 1024, w2s + (k) * 4096, 16)
  if (w < 7) { dma4_sc1(sl2, lane * 16, rsW, w2s + 2 * 4096); dma4_sc1(sl2 + 4096, lane * 16, rsW, w2s + 3 * 4096); VMCNT(8); }
  else VMCNT(0);
  BAR();
  if (t == 7 * 64) set_flag(ws, F_X, idA, tag);
  MARK(22);
  if (w < 7) {
#pragma unroll
    for (int nt = 0; nt < 4; ++nt) W2_LD(0, nt);
#pragma unroll
    for (int nt = 0; nt < 4; ++nt) W2_LD(1, nt);
  }
  const int n0 = 224 * jB + 32 * (w < 7 ? w : 0) + 4 * G;
  const u32x2 bb0 = *(const GAS u32x2*)(b1 + n0), bb1 = *(const GAS u32x2*)(b1 + n0 + 16);
  const u32x4 b2v = *(const GAS u32x4*)(b2 + col0);
  SCHED();
  poll(ws, flag_off(F_X, rbB * 8 + (w == 7 ? (lane & 7) : w)), tag);
  MARK(23);
#pragma unroll
  for (int m = 0; m < 4; ++m) dma2_sc1(L_X1 + (m * 16 + 2 * w) * 1024, lane * 16, rsW, WS_X + ((rbB * 4 + m) * 16 + 2 * w) * 1024);

  if (w < 7) {
    f32x2 uu[4], vv[4];
    {
      f32x4 a0 = {}, a1 = {};
      const lptr p = L + L_MOD + ((tok & 1) ? 3072 : 4096) + G * 16;
      u32x4 f[16];
#pragma unroll
      for (int i = 0; i < 6; ++i) f[i] = lds128(p + i * 64);
      SCHED();
#pragma unroll
      for (int ks = 0; ks < 16; ++ks) {
        if (ks + 6 < 16) f[ks + 6] = lds128(p + (ks + 6) * 64);
        a0 = MFMA16(w1f[0][ks], f[ks], a0); a1 = MFMA16(w1f[1][ks], f[ks], a1);
        SCHED();
      }
#pragma unroll
      for (int r = 0; r < 2; ++r) {
        uu[r] = f32x2{dpp<0xA0>(a0[2 * r]), dpp<0xA0>(a0[2 * r + 1])}; uu[2 + r] = f32x2{dpp<0xA0>(a1[2 * r]), dpp<0xA0>(a1[2 * r + 1])};
        vv[r] = f32x2{dpp<0xF5>(a0[2 * r]), dpp<0xF5>(a0[2 * r + 1])} + up2(bb0[r]);
        vv[2 + r] = f32x2{dpp<0xF5>(a1[2 * r]), dpp<0xF5>(a1[2 * r + 1])} + up2(bb1[r]);
      }
#pragma unroll
      for (int r = 0; r < 4; ++r) { PIN(uu[r]); PIN(vv[r]); }
    }
    VMCNT(4);
    BAR();
    MARK(24);
    auto epi = [&](f32x4 (&acc)[2][2], int half) {
#pragma unroll
      for (int m = 0; m < 2; ++m) {
        const f32x2 st = *(LAS f32x2*)(L + L_STAT + ((2 * half + m) * 16 + tok) * 8);
        u32x4 o;
#pragma unroll
        for (int e = 0; e < 4; ++e) {
          f32x2 v = fma2(bc2(st.x), f32x2{acc[m][e >> 1][2 * (e & 1)], acc[m][e >> 1][2 * (e & 1) + 1]}, fma2(bc2(st.y), uu[e], vv[e]));
          v.x = v.x > 0.f ? v.x : 0.f; v.y = v.y > 0.f ? v.y : 0.f;
          o[e] = pk2(v * v);
        }
        __builtin_amdgcn_raw_buffer_store_b128(o, rW, lane * 16 + WS_H + ((rbB * 4 + 2 * half + m) * 56 + 7 * jB + w) * 1024, 0, 16);
      }
      SCHED();
    };
    constexpr int XD = 3;
    {
      __builtin_amdgcn_s_setprio(1);
      f32x4 acc[2][2] = {};
      const lptr xp = L + L_X1 + lane * 16;
      u32x4 f[16][2];
#pragma unroll
      for (int i = 0; i < XD; ++i) { f[i][0] = lds128(xp + i * 1024); f[i][1] = lds128(xp + 16384 + i * 1024); }
      SCHED();
#pragma unroll
      for (int ks = 0; ks < 16; ++ks) {
#pragma unroll
        for (int m = 0; m < 2; ++m) {
          if (ks + XD < 16) f[ks + XD][m] = lds128(xp + m * 16384 + (ks + XD) * 1024);
          acc[m][0] = MFMA16(w1f[0][ks], f[ks][m], acc[m][0]);
          acc[m][1] = MFMA16(w1f[1][ks], f[ks][m], acc[m][1]);
          SCHED();
        }
      }
      __builtin_amdgcn_s_setprio(0);
      VMCNT(0);
      cnt_inc(C_XB);
      cnt_wait(L, C_STAT, 1);
      MARK(25);
      epi(acc, 0);
    }
    dma4_sc1(sl2x, lane * 16, rsW, w2s + 4 * 4096);
    VMCNT(4);
    if (cnt_add(C_HA) == 6 && lane == 0) set_flag(ws, F_HA, idB, tag);
    cnt_wait(L, C_XB, 8);
    MARK(26);
    {
      f32x4 acc[2][2] = {};
      const lptr xp = L + L_X1 + 32768 + lane * 16;
      u32x4 f[16][2];
#pragma unroll
      for (int i = 0; i < XD; ++i) { f[i][0] = lds128(xp + i * 1024); f[i][1] = lds128(xp + 16384 + i * 1024); }
      SCHED();
#pragma unroll
      for (int ks = 0; ks < 16; ++ks) {
#pragma unroll
        for (int m = 0; m < 2; ++m) {
          if (ks + XD < 16) f[ks + XD][m] = lds128(xp + m * 16384 + (ks + XD) * 1024);
          acc[m][0] = MFMA16(w1f[0][ks], f[ks][m], acc[m][0]);
          acc[m][1] = MFMA16(w1f[1][ks], f[ks][m], acc[m][1]);
          SCHED();
        }
        if (ks < 12) { W2_LD(5 + (ks >> 2), ks & 3); SCHED(); }
      }
      MARK(27);
      epi(acc, 1);
    }
    VMCNT(14);
#pragma unroll
    for (int nt = 0; nt < 4; ++nt) {
      w2f[2][nt] = lds128(L + sl2 + nt * 1024 + lane * 16);
      w2f[3][nt] = lds128(L + sl2 + 4096 + nt * 1024 + lane * 16);
      w2f[4][nt] = lds128(L + sl2x + nt * 1024 + lane * 16);
    }
    cnt_wait(L, C_RA, 1);
    MARK(28);
    u32x4 hf[4][8];
#define H_LD(m, k) hf[m][k] = __builtin_amdgcn_raw_buffer_load_b128(rW, ((k) & 3) * 1024 + lane * 16, WS_H + ((rbA * 4 + (m)) * 56 + 8 * w + ((k) & 4)) * 1024, 16)
#pragma unroll
    for (int k = 0; k < 8; ++k) H_LD(0, k);
#pragma unroll
    for (int k = 0; k < 8; ++k) H_LD(1, k);
    SCHED();
    VMCNT(16);
    if (cnt_add(C_HB) == 6 && lane == 0) set_flag(ws, F_HB, idB, tag);
    SCHED();
#pragma unroll
    for (int m = 0; m < 4; ++m) {
      f32x4 acc[4] = {};
#pragma unroll
      for (int k = 0; k < 8; ++k) {
#pragma unroll
        for (int nt = 0; nt < 4; ++nt) acc[nt] = MFMA16(w2f[k][nt], hf[m][k], acc[nt]);
        if (m == 1) H_LD(3, k);
        SCHED();
      }
      if (m == 0) {
        cnt_wait(L, C_RB, 1);
        MARK(29);
#pragma unroll
        for (int k = 0; k < 8; ++k) H_LD(2, k);
        SCHED();
      }
      const lptr pd = L + (m < 2 ? L_W2 : L_PB) + w * 8192 + (m & 1) * 4096 + lane * 16;
#pragma unroll
      for (int nt = 0; nt < 4; ++nt) *(LAS f32x4*)(pd + nt * 1024) = acc[nt];
      SCHED();
    }
  } else {
    u32x4 sv[8];
#pragma unroll
    for (int k = 0; k < 8; ++k) sv[k] = __builtin_amdgcn_raw_buffer_load_b128(rW, (k & 3) * 1024 + lane * 16, WS_ST + rbB * 8192 + (k & 4) * 1024, 16);
    SCHED();
    VMCNT(12);
    BAR();
    MARK(24);
#pragma unroll
    for (int m = 0; m < 4; ++m) {
      float a[4];
#pragma unroll
      for (int e = 0; e < 4; ++e) a[e] = asf(sv[2 * m][e]) + asf(sv[2 * m + 1][e]);
      float z = add32(add16(a[0], a[1]), add16(a[2], a[3]));
      z += dpp<DPP_ROR8>(z);
      const float o = dpp<DPP_ROR4>(z);
      const float mean = z * (1.f / 512.f);
      const float var = __builtin_fmaxf(__builtin_fmaf(o, 1.f / 512.f, -mean * mean), 0.f);
      const float rs = __builtin_amdgcn_rsqf(var + 1e-5f);
      if ((lane & 12) == 0) *(LAS f32x2*)(L + L_STAT + (m * 16 + 4 * (lane & 3) + G) * 8) = f32x2{rs, -mean * rs};
    }
    cnt_inc(C_STAT);
    VMCNT(0);
    cnt_inc(C_XB);
    MARK(25);
    if (b == 0) {
#pragma unroll
      for (int k = 0; k < 4; ++k) poll(ws, flag_off(F_K, k * 64 + lane), tag);
      if (lane == 0) __hip_atomic_store((GAS u64*)(ws + WS_EPOCH), tag, RLX_AGENT);
    }
    cnt_wait(L, C_HA, 7);
    poll(ws, flag_off(F_HA, rbA * 8 + (lane & 7)), tag);
    cnt_inc(C_RA);
    MARK(28);
    cnt_wait(L, C_HB, 7);
    poll(ws, flag_off(F_HB, rbA * 8 + (lane & 7)), tag);
    cnt_inc(C_RB);
    MARK(29);
  }
  const u32x4 gb = lds128(L + L_MOD + 5120 + col0 * 2);
  BAR();
  MARK(30);
  {
    const lptr p = L + (mt < 2 ? L_W2 : L_PB) + (mt & 1) * 4096 + (2 * kh + (G & 1)) * 1024 + ((2 * (G >> 1)) * 16 + tok) * 16;
    f32x4 s0 = *(LAS f32x4*)p, s1 = *(LAS f32x4*)(p + 256);
#pragma unroll
    for (int v = 1; v < 7; ++v) { s0 += *(LAS f32x4*)(p + v * 8192); s1 += *(LAS f32x4*)(p + v * 8192 + 256); }
    u32x4 o;
#pragma unroll
    for (int e = 0; e < 4; ++e) {
      const f32x2 a = e < 2 ? f32x2{s0[2 * e], s0[2 * e + 1]} : f32x2{s1[2 * e - 4], s1[2 * e - 3]};
      o[e] = pk2(fma2(up2(gb[e]), a + up2(b2v[e]), up2(x1p[e])));
    }
    __builtin_amdgcn_raw_buffer_store_b128(o, __builtin_amdgcn_make_buffer_rsrc(out, 0, 2048 * 1024, 0x00020000), ((rbA * 64 + mt * 16 + tok) * 512 + col0) * 2, 0, 16);
  }
  asm volatile(".Lpf_end:");
}
__global__ void noop_k(char* ws) { if (ws == nullptr) ws[0] = 1; }
}

extern "C" void kernel_launch(void* const* d_in, const int* in_sizes, int n_in, void* d_out, int out_size, void* d_ws, size_t ws_size, hipStream_t stream) {
  fused_block<<<256, 512, 0, stream>>>((const u16*)d_in[0], (const u16*)d_in[1], (const u16*)d_in[2], (const u16*)d_in[4], (const u16*)d_in[5], (const u16*)d_in[6],
                                       (const u16*)d_in[7], (const u16*)d_in[8], (const u16*)d_in[9], (u16*)d_out, (char*)d_ws);
}
```

```cpp
#include <hip/hip_runtime.h>
#include <hip/hip_bf16.h>
#include <stdint.h>

#define PROF 0
#define BG 31
#define NAT 8
#define NPO 11
#define WO0 8
#define OD 16
#define GW 16
#define TOUCH0 0

namespace {
typedef unsigned short u16;
typedef unsigned u32;
typedef unsigned long long u64;
typedef float f32x2 __attribute__((ext_vector_type(2)));
typedef float f32x4 __attribute__((ext_vector_type(4)));
typedef float f32x16 __attribute__((ext_vector_type(16)));
typedef u32 u32x2 __attribute__((ext_vector_type(2)));
typedef u32 u32x4 __attribute__((ext_vector_type(4)));
typedef short bf16x8 __attribute__((ext_vector_type(8)));
typedef short s16x4 __attribute__((ext_vector_type(4)));
typedef __bf16 bf16x2_t __attribute__((ext_vector_type(2)));
#define GAS __attribute__((address_space(1)))
#define LAS __attribute__((address_space(3)))

constexpr u32 WS_EPOCH = 0, WS_MODG = 4096, WS_WFLAG = 32768, WS_MODE = 65536, WS_VOTE = 98304, WS_MODP = 131072, WS_PROBE = 1u << 20;
constexpr u32 WS_W1T = 4u << 20, WS_W2T = 8u << 20, WS_HB = 12u << 20, WS_PROF = 28u << 20, WS_END = 32u << 20;
enum { CH_K = 0, CH_V = 8, CH_O = 16, CH_X = 24, CH_H = 32, CH_ST = 60, CH_FL = 61 };
enum { F_K = 0, F_V, F_O, F_X, F_HA, F_HB };
constexpr u32 L_H = 0, L_STG = 65536, L_RT = 131072, L_LM = 147456;
constexpr u32 L_KIMG = 0, L_VIMG = 40960, L_WO = 81920, L_Q = 40960;
constexpr u32 L_X1 = 0, L_XCH = 65536, L_W2 = 65536, L_PB = 0;
constexpr u32 L_MOD = 155648, L_B1 = L_MOD, L_B2 = L_MOD + 1024, L_LC = L_MOD + 4096, L_MISC = 161792;
constexpr u32 L_PART = L_MISC, L_STAT = L_MISC + 1024, L_ONES = L_MISC + 1024, L_CNT = L_MISC + 1696, L_DUMMY = L_MISC + 1792;
enum { C_K = 0, C_WQ, C_XB, C_HA, C_HB, C_STAT, C_RA, C_RB, C_MOD, C_WK, C_WV, C_KH, C_VH, C_WF, C_V, C_K4, C_POL, C_T0 = 20 };
constexpr u32 LDS_BYTES = 163840;
constexpr int SPIN_CAP = 1 << 18;
constexpr float QSCALE = 0.25f * 1.4426950408889634f;

__device__ __forceinline__ u32 pk(float a, float b) { f32x2 v = {a, b}; return __builtin_bit_cast(u32, __builtin_convertvector(v, bf16x2_t)); }
__device__ __forceinline__ float lo16(u32 v) { return __builtin_bit_cast(float, v << 16); }
__device__ __forceinline__ float hi16(u32 v) { return __builtin_bit_cast(float, v & 0xffff0000u); }
__device__ __forceinline__ float asf(u32 v) { return __builtin_bit_cast(float, v); }
__device__ __forceinline__ u32 asu(float v) { return __builtin_bit_cast(u32, v); }
__device__ __forceinline__ f32x2 up2(u32 v) { return f32x2{lo16(v), hi16(v)}; }
__device__ __forceinline__ u32 pk2(f32x2 v) { return __builtin_bit_cast(u32, __builtin_convertvector(v, bf16x2_t)); }
__device__ __forceinline__ f32x2 fma2(f32x2 a, f32x2 b, f32x2 c) { return __builtin_elementwise_fma(a, b, c); }
__device__ __forceinline__ f32x2 bc2(float a) { return f32x2{a, a}; }
__device__ __forceinline__ float dot2(u32 a, u32 b, float c) { return __builtin_amdgcn_fdot2_f32_bf16(__builtin_bit_cast(bf16x2_t, a), __builtin_bit_cast(bf16x2_t, b), c, false); }
template <int CTRL> __device__ __forceinline__ float dpp(float v) { return asf((u32)__builtin_amdgcn_update_dpp(0, (int)asu(v), CTRL, 0xF, 0xF, true)); }
__device__ __forceinline__ float add16(float a, float b) { auto r = __builtin_amdgcn_permlane16_swap(asu(a), asu(b), false, false); float v = asf(r[0]) + asf(r[1]); asm("" : "+v"(v)); return v; }
__device__ __forceinline__ float add32(float a, float b) { auto r = __builtin_amdgcn_permlane32_swap(asu(a), asu(b), false, false); float v = asf(r[0]) + asf(r[1]); asm("" : "+v"(v)); return v; }
constexpr int DPP_X1 = 0xB1, DPP_X2 = 0x4E, DPP_ROR4 = 0x124, DPP_ROR8 = 0x128;

__device__ __forceinline__ u32x4 mkrs(const void* p, u32 bytes) { return u32x4{(u32)(uintptr_t)p, (u32)((uintptr_t)p >> 32), bytes, 0x00020000u}; }
#define DMA_L(o) "buffer_load_dwordx4 %2, %3, %0 offen offset:" #o " lds\n\t"
#define DMA_A "s_add_u32 %0, %0, %4\n\t"
#define DMA_G DMA_L(0) DMA_A DMA_L(1024) DMA_A DMA_L(2048) DMA_A DMA_L(3072)
#define DMA_N "s_add_u32 m0, m0, 0x1000\n\ts_add_u32 %0, %0, %5\n\t"
__device__ __forceinline__ void dma4v(u32 lds, u32 voff, u32x4 rs, u32 soff, u32 stride) {
  asm volatile("s_mov_b32 m0, %1\n\ts_nop 0\n\t" DMA_G : "+&s"(soff) : "s"(lds), "v"(voff), "s"(rs), "s"(stride - 1024) : "memory", "scc");
}
template <int STRIDE> __device__ __forceinline__ void dma8(u32 lds, u32 voff, u32x4 rs, u32 soff) {
  asm volatile("s_mov_b32 m0, %1\n\ts_nop 0\n\t" DMA_G DMA_N DMA_G : "+&s"(soff) : "s"(lds), "v"(voff), "s"(rs), "s"(STRIDE - 1024), "i"(STRIDE + 3072) : "memory", "scc");
}
__device__ __forceinline__ void dma1(u32 lds, u32 voff, u32x4 rs, u32 soff) {
  asm volatile("s_mov_b32 m0, %0\n\ts_nop 0\n\tbuffer_load_dwordx4 %1, %2, %3 offen lds" ::"s"(lds), "v"(voff), "s"(rs), "s"(soff) : "memory");
}
#define DMS_L(o) "buffer_load_dwordx4 %1, %2, %3 offen offset:" #o " sc1 lds\n\t"
__device__ __forceinline__ void dma4_sc1(u32 lds, u32 voff, u32x4 rs, u32 soff) {
  asm volatile("s_mov_b32 m0, %0\n\ts_nop 0\n\t" DMS_L(0) DMS_L(1024) DMS_L(2048) DMS_L(3072)::"s"(lds), "v"(voff), "s"(rs), "s"(soff) : "memory");
}
__device__ __forceinline__ void dma2_sc1(u32 lds, u32 voff, u32x4 rs, u32 soff) {
  asm volatile("s_mov_b32 m0, %0\n\ts_nop 0\n\t" DMS_L(0) DMS_L(1024)::"s"(lds), "v"(voff), "s"(rs), "s"(soff) : "memory");
}
__device__ __forceinline__ void touch(u32 voff, u32x4 rs, u32 soff) {
  asm volatile("s_mov_b32 m0, %0\n\ts_nop 0\n\tbuffer_load_dword %1, %2, %3 offen lds" ::"s"(L_DUMMY), "v"(voff), "s"(rs), "s"(soff) : "memory");
}
#define VMCNT(n) asm volatile("s_waitcnt vmcnt(%0)" ::"i"(n) : "memory")
#define VMCNT0() do { asm volatile("s_waitcnt vmcnt(0)" ::: "memory"); __builtin_amdgcn_s_waitcnt(0x0F70); } while (0)
#define BAR() asm volatile("s_waitcnt lgkmcnt(0)\n\ts_barrier" ::: "memory")
#define SCHED() __builtin_amdgcn_sched_barrier(0)
#define PIN(x) asm volatile("" : "+v"(x))
#define PINS(x) asm volatile("" : "+s"(x))
#define UNDEF(x) asm volatile("" : "=v"(x))
#define MFMA32(a, b, c) __builtin_amdgcn_mfma_f32_32x32x16_bf16(__builtin_bit_cast(bf16x8, a), __builtin_bit_cast(bf16x8, b), c, 0, 0, 0)
#define MFMA16(a, b, c) __builtin_amdgcn_mfma_f32_16x16x32_bf16(__builtin_bit_cast(bf16x8, a), __builtin_bit_cast(bf16x8, b), c, 0, 0, 0)
__device__ __forceinline__ float sc(float v) { asm("" : "+v"(v)); return v; }
__device__ __forceinline__ int opaque(int v) { asm volatile("" : "+s"(v)); return v; }

typedef LAS char* lptr;
__device__ __forceinline__ u32x4 lds128(lptr p) { return *(LAS u32x4*)p; }
__device__ __forceinline__ u32x2 ldstr(lptr p) { return __builtin_bit_cast(u32x2, __builtin_amdgcn_ds_read_tr16_b64_v4i16((LAS s16x4*)p)); }
__device__ __forceinline__ u32x4 trfrag(lptr p, int o0, int o1) { u32x2 a = ldstr(p + o0), b = ldstr(p + o1); return u32x4{a.x, a.y, b.x, b.y}; }

__device__ __forceinline__ void set_flag(u32x4 rs, u32 voff, u32 soff, u64 tag, u64 mask) {
  asm volatile("s_mov_b64 exec, %4\n\tbuffer_store_dwordx2 %0, %1, %2, %3 offen sc1\n\ts_mov_b64 exec, -1" ::"v"(tag), "v"(voff), "s"(rs), "s"(soff), "s"(mask) : "memory");
}
__device__ __forceinline__ u64 peek(u32x4 rs, u32 voff, u32 soff, u64 mask) {
  u64 f;
  asm volatile("s_mov_b64 exec, %4\n\tbuffer_load_dwordx2 %0, %1, %2, %3 offen sc1\n\ts_mov_b64 exec, -1" : "=&v"(f) : "v"(voff), "s"(rs), "s"(soff), "s"(mask) : "memory");
  return f;
}
template <bool WAIT = true> __device__ __forceinline__ void poll_rest(u32x4 rs, u32 voff, u32 soff, u64 tag, u64 mask, u64 f) {
  u32 left;
  asm volatile("s_mov_b64 exec, %6\n\ts_mov_b32 %1, 0x40000\n\ts_waitcnt vmcnt(%7)\n"
               "1:\n\tv_cmp_ne_u64 vcc, %5, %0\n\ts_cbranch_vccz 2f\n\ts_sub_u32 %1, %1, 1\n\ts_cbranch_scc1 2f\n\t"
               "buffer_load_dwordx2 %0, %2, %3, %4 offen sc1\n\ts_waitcnt vmcnt(0)\n\ts_branch 1b\n"
               "2:\n\ts_mov_b64 exec, -1"
               : "+v"(f), "=&s"(left) : "v"(voff), "s"(rs), "s"(soff), "s"(tag), "s"(mask), "i"(WAIT ? 0 : 63) : "memory", "vcc", "scc");
  if (WAIT) __builtin_amdgcn_s_waitcnt(0x0F70);
}
__device__ __forceinline__ void poll(u32x4 rs, u32 voff, u32 soff, u64 tag, u64 mask) { poll_rest(rs, voff, soff, tag, mask, peek(rs, voff, soff, mask)); }
__device__ __forceinline__ u32 hbase(int rb, int pol) { return WS_HB + (u32)(rb & 15) * (1u << 20) + (u32)(((rb >> 4) ^ (rb >> 1) ^ pol) & 1) * 8192u; }
__device__ __forceinline__ u32 fl_off(u32 hb, int ph, int j) { return hb + CH_FL * 16384 + (ph * 8 + j) * 128; }

#define cnt_inc(i) asm volatile("s_mov_b64 exec, 1\n\tds_add_u32 %0, %1 offset:%2\n\ts_mov_b64 exec, -1" ::"v"(cb), "v"(one), "i"(4 * (i)) : "memory")
#define cnt_flag(i, last, voff, soff, mask) do { u32 o_;                                                                                             \
    asm volatile("s_mov_b64 exec, 1\n\tds_add_rtn_u32 %0, %1, %2 offset:%3\n\ts_waitcnt lgkmcnt(0)\n\tv_cmp_eq_u32 vcc, %4, %0\n\ts_cbranch_vccz 1f\n\t" \
                 "s_mov_b64 exec, %9\n\tbuffer_store_dwordx2 %5, %6, %7, %8 offen sc1\n1:\n\ts_mov_b64 exec, -1"                                     \
                 : "=&v"(o_) : "v"(cb), "v"(one), "i"(4 * (i)), "i"(last), "v"(tag), "v"(voff), "s"(rsW), "s"(soff), "s"((u64)(mask)) : "memory", "vcc"); } while (0)
#define cnt_wait(i, n) do { u32 v_, l_;                                                                                                               \
    asm volatile("s_mov_b32 %1, 0x200000\n1:\n\tds_read_b32 %0, %2 offset:%3\n\ts_sub_u32 %1, %1, 1\n\ts_cbranch_scc1 2f\n\ts_waitcnt lgkmcnt(0)\n\t"   \
                 "v_cmp_gt_u32 vcc, %4, %0\n\ts_cbranch_vccnz 1b\n2:\n\ts_waitcnt lgkmcnt(0)"                                                        \
                 : "=&v"(v_), "=&s"(l_) : "v"(cb), "i"(4 * (i)), "i"(n) : "memory", "vcc", "scc"); } while (0)

__device__ __forceinline__ float silu_bf(float cf) {
  const float s = cf * __builtin_amdgcn_rcpf(1.f + __builtin_amdgcn_exp2f(-1.4426950408889634f * cf));
  return lo16(pk(s, s));
}
__device__ __forceinline__ void fold8(const f32x2 (&a)[4], float& c0, float& c1) {
  c0 = add32(add16(a[0].x, a[0].y), add16(a[1].x, a[1].y)); c1 = add32(add16(a[2].x, a[2].y), add16(a[3].x, a[3].y));
  c0 += dpp<DPP_ROR8>(c0); c1 += dpp<DPP_ROR8>(c1);
}
template <int SH> __device__ __forceinline__ u32 gran_bad(u32x4 g, u64 tag) {
  const u32 t0 = (u32)tag << SH, t1 = (u32)(tag >> (32 - SH));
  return (((g.x ^ t0) | (g.z ^ t0)) >> SH) | (g.y ^ t1) | (g.w ^ t1);
}

#define MARK(i) do {} while (0)

__global__ void __launch_bounds__(512, 2)
fused_block(const u16* x_, const u16* wmod_, const u16* c_, char* ws_, const u16* wqkv_, const u16* w1_,
            const u16* w2_, const u16* wo_, const u16* b1_, const u16* b2_, u16* out_) {
  __shared__ __attribute__((aligned(16))) char smem[LDS_BYTES];
  const lptr L = (lptr)smem;
  MARK(0);
  const u16 *x, *wmod, *c, *wqkv;
  char* ws0;
  u64 prt;
  asm volatile("s_load_dwordx2 %0, %6, 0x0\n\ts_load_dwordx2 %1, %6, 0x8\n\ts_load_dwordx2 %2, %6, 0x10\n\ts_load_dwordx2 %3, %6, 0x18\n\ts_load_dwordx2 %4, %6, 0x20\n\ts_memrealtime %5"
               : "=&s"(x), "=&s"(wmod), "=&s"(c), "=&s"(ws0), "=&s"(wqkv), "=&s"(prt) : "s"(__builtin_amdgcn_kernarg_segment_ptr()));
  SCHED();
  const int b = blockIdx.x, xcd = b & 7, bi = b >> 3;
  const int t = threadIdx.x, lane = t & 63, w = __builtin_amdgcn_readfirstlane(t >> 6);
  int rbA = xcd * 4 + (bi >> 3), jA = bi & 7;
  int rbB = (xcd >> 1) * 8 + (bi >> 2), jB = (xcd & 1) * 4 + (bi & 3);
  u32 l16 = lane * 16;
  {
    const u32 v = (u32)((lane >> 2) - (8 - 2 * rbB)) < (u32)(2 + 2 * rbB) ? 0x3f803f80u : 0u;
    *(LAS u32*)(L + L_ONES + 4 * lane) = v;
    *(LAS u32*)(L + L_ONES + 512 + 4 * lane) = v;
  }
  const int wr = w ^ (4 * (jB & 1));
  u32 xso = (u32)(rbB * 64 + wr * 8) * 1024, mso = (u32)(b >> 4) * 256 * 6144 + (b & 15) * 128, mvo = (u32)(t >> 3) * (8 * 6144) + (t & 7) * 16, cvo = (u32)t * 2;
  u32 cso = (b >> 4) * 512, mcond = b | ((w >> 2) << 5), pof = WS_PROBE + (((b - 32) >> 4) * 8 + xcd) * 16384 + (bi & 1) * 8192;
  PINS(pof); PINS(xso); PINS(mso); PINS(cso); PINS(mcond); PIN(l16); PIN(mvo); PIN(cvo);
  u32 wl0, wg0, wg1, wvoff;
  {
    const int rot = rbB & 1;
    wl0 = 16384 * (w & 3) + 8192 * rot; wg0 = (128 * (w & 3) + 64 * rot) * 3072; wg1 = (128 * (w & 3) + 64 * (rot ^ 1)) * 3072;
    wvoff = (l16 >> 7) * 3072 + jB * 128 + ((l16 & 112) ^ ((l16 >> 2) & 64));
  }
  PINS(wl0); PINS(wg0); PINS(wg1); PIN(wvoff);
  asm volatile("s_waitcnt lgkmcnt(0)\n\ts_barrier" : "+s"(x), "+s"(wmod), "+s"(c), "+s"(ws0), "+s"(wqkv), "+s"(prt)::"memory");
  const u32x4 rsQ = mkrs(wqkv, 512u * 3072u), rsX = mkrs(x, 2048u * 1024u);
#define LANE() PIN(l16); const int lane = l16 >> 4, G = lane >> 4, q4 = (lane >> 2) & 3, pp = lane & 3, r32 = lane & 31, hh = lane >> 5, tok = lane & 15, t = lane + 64 * w; \
               (void)G; (void)q4; (void)pp; (void)r32; (void)hh; (void)tok; (void)t

  u32 cv;
  u32x4 wm[8];
  asm volatile("s_cmp_lt_u32 %15, 32\n\ts_cbranch_scc0 .Lnomod\n\t"
               "buffer_load_ushort %8, %12, %13, %14 offen\n\t"
               "buffer_load_dwordx4 %0, %10, %11, %9 offen\n\ts_addk_i32 %9, 0x1800\n\t"
               "buffer_load_dwordx4 %1, %10, %11, %9 offen\n\ts_addk_i32 %9, 0x1800\n\t"
               "buffer_load_dwordx4 %2, %10, %11, %9 offen\n\ts_addk_i32 %9, 0x1800\n\t"
               "buffer_load_dwordx4 %3, %10, %11, %9 offen\n\ts_addk_i32 %9, 0x1800\n\t"
               "buffer_load_dwordx4 %4, %10, %11, %9 offen\n\ts_addk_i32 %9, 0x1800\n\t"
               "buffer_load_dwordx4 %5, %10, %11, %9 offen\n\ts_addk_i32 %9, 0x1800\n\t"
               "buffer_load_dwordx4 %6, %10, %11, %9 offen\n\ts_addk_i32 %9, 0x1800\n\t"
               "buffer_load_dwordx4 %7, %10, %11, %9 offen\n"
               ".Lnomod:"
               : "=&v"(wm[0]), "=&v"(wm[1]), "=&v"(wm[2]), "=&v"(wm[3]), "=&v"(wm[4]), "=&v"(wm[5]), "=&v"(wm[6]), "=&v"(wm[7]), "=&v"(cv), "+&s"(mso)
               : "v"(mvo), "s"(mkrs(wmod, 512u * 6144u)), "v"(cvo), "s"(mkrs(c, 1024u)), "s"(cso), "s"(mcond)
               : "memory", "scc");
  if (w == 7) {
    u64 pc;
    u32 len;
    asm volatile("s_getpc_b64 %0\n.Lpf_here:\n\ts_mov_b32 %1, .Lpf_end-.Lpf_here" : "=s"(pc), "=s"(len));
    touch(l16 * 8, mkrs((const void*)pc, len), (u32)(bi & 3) * 8192);
  }
  char* ws = (char*)(((uintptr_t)ws0 + 0x1fffff) & ~(uintptr_t)0x1fffff);
  PINS(ws);
  MARK(1);
  const bool voter = b >= 32 && b < 160 && w == 0;
  u64 pt0;
  u32 pd;
  if (voter) {
    asm volatile("s_memtime %0\n\ts_load_dword %1, %2, %3" : "=&s"(pt0), "=&s"(pd) : "s"(ws), "s"(pof + (((u32)prt >> 3) & 0x1f80)) : "memory");
  }
  u32x4 xr[8];
#define X_LOADS "buffer_load_dwordx4 %0, %8, %9, %10 offen\n\t"                 \
               "buffer_load_dwordx4 %1, %8, %9, %10 offen offset:1024\n\t"     \
               "buffer_load_dwordx4 %2, %8, %9, %10 offen offset:2048\n\t"     \
               "buffer_load_dwordx4 %3, %8, %9, %10 offen offset:3072\n\t"     \
               "buffer_load_dwordx4 %4, %8, %9, %11 offen\n\t"                 \
               "buffer_load_dwordx4 %5, %8, %9, %11 offen offset:1024\n\t"     \
               "buffer_load_dwordx4 %6, %8, %9, %11 offen offset:2048\n\t"     \
               "buffer_load_dwordx4 %7, %8, %9, %11 offen offset:3072\n1:"
  asm volatile(X_LOADS
               : "=&v"(xr[0]), "=&v"(xr[1]), "=&v"(xr[2]), "=&v"(xr[3]), "=&v"(xr[4]), "=&v"(xr[5]), "=&v"(xr[6]), "=&v"(xr[7])
               : "v"(l16), "s"(rsX), "s"(xso), "s"(xso + 4096) : "memory");
  u32 pdt = 0;
  if (voter) {
    u64 t1;
    asm volatile("s_waitcnt lgkmcnt(0)\n\ts_memtime %0\n\ts_waitcnt lgkmcnt(0)" : "=s"(t1), "+s"(pd), "+s"(pt0)::"memory");
    pdt = (u32)t1 - (u32)pt0;
  }
  u64 tag;
  const u16 *w1, *w2, *wo, *b1, *b2;
  u16* out;
  asm volatile("s_load_dwordx2 %0, %7, 0x0\n\ts_load_dwordx2 %1, %8, 0x28\n\ts_load_dwordx2 %2, %8, 0x30\n\ts_load_dwordx2 %3, %8, 0x38\n\t"
               "s_load_dwordx2 %4, %8, 0x40\n\ts_load_dwordx2 %5, %8, 0x48\n\ts_load_dwordx2 %6, %8, 0x50"
               : "=&s"(tag), "=&s"(w1), "=&s"(w2), "=&s"(wo), "=&s"(b1), "=&s"(b2), "=&s"(out) : "s"(ws), "s"(__builtin_amdgcn_kernarg_segment_ptr()) : "memory");
  if (w >= 4) {
    if (b >= 32) __builtin_amdgcn_s_setprio(1);
    dma8<8 * 3072>(L_STG + wl0, wvoff, rsQ, 2048 + wg0);
    dma8<8 * 3072>(L_STG + (wl0 ^ 8192), wvoff, rsQ, 2048 + wg1);
    __builtin_amdgcn_s_setprio(0);
  }
  const u32x4 rsW = mkrs(ws, WS_END);
  const __amdgpu_buffer_rsrc_t rW = __builtin_amdgcn_make_buffer_rsrc(ws, 0, WS_END, 0x00020000);
  u32 cb = L_CNT, one = 1, zero0 = 0;
  PIN(cb); PIN(one); PIN(zero0);
  MARK(2);
  if (opaque(w) >= 4) {
    VMCNT(16);
  } else if (b < 32) {
    LANE();
    asm volatile("s_waitcnt vmcnt(16)" : "+v"(cv)::"memory");
    *(LAS float*)(L + L_LC + t * 4) = silu_bf(lo16(cv));
    const f32x4 sa = *(LAS f32x4*)(L + L_LC + (t >> 3) * 32), sb = *(LAS f32x4*)(L + L_LC + (t >> 3) * 32 + 16);
    const float sl[8] = {sa[0], sa[1], sa[2], sa[3], sb[0], sb[1], sb[2], sb[3]};
    f32x2 a[4];
#pragma unroll
    for (int i = 0; i < 8; ++i) {
      asm volatile("s_waitcnt vmcnt(%1)" : "+v"(wm[i]) : "i"(15 - i) : "memory");
#pragma unroll
      for (int e = 0; e < 4; ++e) { a[e] = i == 0 ? bc2(sl[i]) * up2(wm[i][e]) : fma2(bc2(sl[i]), up2(wm[i][e]), a[e]); PIN(a[e]); }
      SCHED();
    }
    float c0, c1;
    fold8(a, c0, c1);
    LAS float* p = (LAS float*)(L + L_PART);
    if ((lane & 8) == 0) { p[w * 64 + 8 * (lane & 7) + G] = c0; p[w * 64 + 8 * (lane & 7) + G + 4] = c1; }
    u32 old;
    asm volatile("s_mov_b64 exec, 1\n\tds_add_rtn_u32 %0, %1, %2 offset:%3\n\ts_mov_b64 exec, -1\n\ts_waitcnt lgkmcnt(0)" : "=&v"(old) : "v"(cb), "v"(one), "i"(4 * C_MOD) : "memory");
    if (__builtin_amdgcn_readfirstlane(old) == 3) {
      const float s = (p[lane] + p[64 + lane]) + (p[128 + lane] + p[192 + lane]);
      u64 tg;
      asm volatile("s_mov_b64 %0, %1" : "=s"(tg) : "s"(tag));
      const u64 gr = ((tg + 1) << 24) | (u64)((asu(s) + 0x80u) >> 8);
      __builtin_amdgcn_raw_buffer_store_b64(__builtin_bit_cast(u32x2, gr), rW, lane * 8, WS_MODE + ((b >> 4) * 1024 + (b & 15) * 64) * 8, 16);
      VMCNT(1);
    } else {
      VMCNT(0);
    }
  } else {
    VMCNT(0);
  }
  asm volatile("" : "+v"(xr[0]), "+v"(xr[1]), "+v"(xr[2]), "+v"(xr[3]), "+v"(xr[4]), "+v"(xr[5]), "+v"(xr[6]), "+v"(xr[7])::"memory");
  MARK(3);
  f32x2 xn[8][4];
  {
    float s1[8], s2[8];
#pragma unroll
    for (int r = 0; r < 8; ++r) {
      s1[r] = dot2(xr[r][3], 0x3f803f80u, dot2(xr[r][2], 0x3f803f80u, dot2(xr[r][1], 0x3f803f80u, dot2(xr[r][0], 0x3f803f80u, 0.f))));
      s2[r] = dot2(xr[r][3], xr[r][3], dot2(xr[r][2], xr[r][2], dot2(xr[r][1], xr[r][1], dot2(xr[r][0], xr[r][0], 0.f))));
    }
#pragma unroll
    for (int r = 0; r < 8; ++r) { PIN(s1[r]); PIN(s2[r]); }
    asm volatile("s_waitcnt lgkmcnt(0)" : "+s"(tag), "+s"(w1), "+s"(w2), "+s"(wo), "+s"(b1), "+s"(b2), "+s"(out)::"memory");
    tag += 1;
    PINS(tag);
    if (voter) set_flag(rsW, zero0, WS_VOTE + (((b - 32) >> 4) * 8 + xcd) * 16 + (bi & 1) * 8, (tag << 16) | (u64)(pdt & 0xffffu), 1);
    float z[4];
    z[0] = add32(add16(s1[0], s1[1]), add16(s1[2], s1[3])); z[1] = add32(add16(s1[4], s1[5]), add16(s1[6], s1[7]));
    z[2] = add32(add16(s2[0], s2[1]), add16(s2[2], s2[3])); z[3] = add32(add16(s2[4], s2[5]), add16(s2[6], s2[7]));
#pragma unroll
    for (int i = 0; i < 4; ++i) z[i] += dpp<DPP_X1>(z[i]);
#pragma unroll
    for (int i = 0; i < 4; ++i) z[i] += dpp<DPP_X2>(z[i]);
#pragma unroll
    for (int i = 0; i < 4; ++i) z[i] += dpp<DPP_ROR4>(z[i]);
#pragma unroll
    for (int i = 0; i < 4; ++i) z[i] += dpp<DPP_ROR8>(z[i]);
    float rs[2], nm[2];
#pragma unroll
    for (int i = 0; i < 2; ++i) {
      const float mean = z[i] * (1.f / 512.f);
      rs[i] = __builtin_amdgcn_rsqf(__builtin_fmaxf(__builtin_fmaf(z[2 + i], 1.f / 512.f, -mean * mean), 0.f) + 1e-5f);
      nm[i] = -mean * rs[i];
    }
#pragma unroll
    for (int r = 0; r < 8; ++r) {
      const float a = asf(__builtin_amdgcn_readlane(asu(rs[r >> 2]), (r & 3) * 16));
      float o = asf(__builtin_amdgcn_readlane(asu(nm[r >> 2]), (r & 3) * 16));
      PIN(o);
#pragma unroll
      for (int e = 0; e < 4; ++e) { xn[r][e] = fma2(up2(xr[r][e]), bc2(a), bc2(o)); PIN(xn[r][e]); }
    }
  }
  MARK(4);
  const int pr = w & 1, th = (w >> 1) & 1;
  const int head = w & 3, qh = w >> 2, mt = w >> 1, kh = w & 1;
  u32x4 wf[32], g[5];
  u32 wtr, t16 = l16 + w * 1024;
  PIN(t16);
  if (w < 4) { dma8<8 * 3072>(wl0, wvoff, rsQ, 1024 + wg0); dma8<8 * 3072>(wl0 ^ 8192, wvoff, rsQ, 1024 + wg1); }
#define G_LOADS "buffer_load_dwordx4 %0, %5, %7, %8 offen sc1\n\tbuffer_load_dwordx4 %1, %5, %7, %9 offen sc1\n\t"   \
                "buffer_load_dwordx4 %2, %5, %7, %10 offen sc1\n\tbuffer_load_dwordx4 %3, %5, %7, %11 offen sc1\n\t" \
                "s_cmp_eq_u32 %13, 3\n\ts_cbranch_scc0 1f\n\tbuffer_load_dwordx4 %4, %6, %7, %12 offen sc1\n1:"
#define G_IN "v"(t16), "v"(l16), "s"(rsW), "s"(WS_MODE), "s"(WS_MODE + 4096), "s"(WS_MODE + 8192), "s"(WS_MODE + 12288), "s"(WS_VOTE), "s"(w)
  {
    LANE();
    wtr = (u32)(8 * (G >> 1) + q4) * 128 + (u32)((64 * pr) ^ (64 * ((q4 >> 1) & 1))) + 32 * (G & 1) + 8 * pp;
    PIN(wtr);
  }
  MARK(5);
  if (opaque(w) >= 4) {
    VMCNT(0);
    cnt_inc(C_WV);
    cnt_wait(C_WV, 4);
    MARK(6);
#pragma unroll
    for (int ks = 0; ks < 32; ++ks) wf[ks] = trfrag(L + L_STG + wtr, ks * 2048, ks * 2048 + 512);
    SCHED();
  }
  u32 hw, lo, jB14, jBf, w10, hsz, hbA, hbB, hfo, hso_, w1s;
  lptr hb[4], mp;
  {
    PINS(rbA); PINS(jA); PINS(rbB); PINS(jB);
    hbA = hbase(rbA, 0); hbB = hbase(rbB, 0); jB14 = jB * 16384; jBf = CH_FL * 16384 + jB * 128; w10 = w * 1024;
    const int prb = rbB - 4 + (w & 3);
    hsz = prb < 0 ? 0u : WS_END; hfo = hbase(prb < 0 ? rbB : prb, 0); hso_ = hfo + jB14 + (w < 4 ? CH_V : CH_K) * 16384; hfo += jBf + (w < 4 ? F_V : F_K) * 1024;
    const int u1 = w < 7 ? 7 * jB + w : 7 * jA + (rbA & 3);
    w1s = (w < 7 ? WS_W1T : WS_W2T) + u1 * 65536 + (((rbA >> 4) ^ (u1 >> 5)) & 1) * 8192;
    PINS(hbA); PINS(hbB); PINS(jB14); PINS(jBf); PINS(w10); PINS(hsz); PINS(hfo); PINS(hso_); PINS(w1s);
    LANE();
    hw = L_H + (wr >> 2) * 32768 + lane * 512 + (8 * (wr & 3) + (lane & 7)) * 16;
    lo = (th * 4 + 2 * pr) * 1024 + (w < 4 ? l16 : ((l16 & 256) << 2) + ((l16 >> 1) & 256) + (l16 & 240));
    mp = L + L_MOD + l16;
    PIN(hw); PIN(lo); PIN(mp);
    const u32 h0 = L_H + th * 32768 + (l16 ^ (hh * 16));
#pragma unroll
    for (int i = 0; i < 4; ++i) { hb[i] = L + (h0 ^ (32 * i)); PIN(hb[i]); }
  }
  const bool isw1 = b < 144;
  MARK(7);
  if (opaque(w) < 4) {
    lptr p = L + L_MOD + ((t16 >> 5) & 1) * 1024 + (t16 >> 6) * 16 + ((t16 >> 4) & 1) * 8;
    PIN(p);
#define G_OK() (__all((gran_bad<24>(g[0], tag) | gran_bad<24>(g[1], tag) | gran_bad<24>(g[2], tag) | gran_bad<24>(g[3], tag) | (w == 3 ? gran_bad<16>(g[4], tag) : 0u)) == 0))
#define G_USE() do { *(LAS f32x2*)p = up2(pk(asf(g[0].x << 8) + asf(g[2].x << 8), asf(g[0].z << 8) + asf(g[2].z << 8)));                                       \
      *(LAS f32x2*)(p + 2048) = up2(pk2(up2(pk(asf(g[1].x << 8) + asf(g[3].x << 8), asf(g[1].z << 8) + asf(g[3].z << 8))) + bc2(1.f)));                 \
                                                                                                              \
      if (w == 3) *(LAS u32*)(L + L_CNT + 4 * C_POL) = __builtin_popcountll(__builtin_amdgcn_ballot_w64((g[4].x & 0xffffu) > (g[4].z & 0xffffu)) ^ 0xf0f0f0f0f0f0f0f0ull); } while (0)
    VMCNT(GW);
    asm volatile(G_LOADS : "=&v"(g[0]), "=&v"(g[1]), "=&v"(g[2]), "=&v"(g[3]), "=&v"(g[4]) : G_IN : "memory", "scc");
    if (w == 3) VMCNT(5); else VMCNT(4);
    cnt_inc(C_WK);
    cnt_wait(C_WK, 4);
    MARK(6);
#pragma unroll
    for (int ks = 0; ks < 32; ++ks) wf[ks] = trfrag(L + wtr, ks * 2048, ks * 2048 + 512);
    SCHED();
    asm volatile("s_waitcnt vmcnt(0)" : "+v"(g[0]), "+v"(g[1]), "+v"(g[2]), "+v"(g[3]), "+v"(g[4])::"memory");
#pragma clang loop unroll(disable)
    for (int s = 0; s < SPIN_CAP; ++s) {
      if (G_OK()) break;
      asm volatile(G_LOADS "\n\ts_waitcnt vmcnt(0)" : "+v"(g[0]), "+v"(g[1]), "+v"(g[2]), "+v"(g[3]), "+v"(g[4]) : G_IN : "memory", "scc");
    }
    G_USE();
  } else {
    LANE();
    if (w == 5) touch(l16 * 192 + jB * 128, rsQ, (u32)(rbB & 7) * 64 * 3072);
    if ((BG & 1) && b >= 32) {
      const int P = opaque(b) - 32, j2 = (P - 112) / 14, ks2 = 4 * (P - 112 - 14 * j2) + (w & 3), ks1 = 4 * (P & 3) + (w & 3);
      const u32 rowb = isw1 ? 3584 : 1024, rso = isw1 ? 32 * ks1 * 3584 + (P >> 2) * 128 : 32 * ks2 * 1024 + j2 * 128;
      dma4v(L_RT - 16384 + w10 * 4, (u32)(lane >> 3) * rowb + (lane & 7) * 16, mkrs(isw1 ? w1 : w2, 1792u * 1024u), rso, 8 * rowb);
    }
    if ((BG & 16) && w == 7) {
      dma8<8 * 6144>(L_LM, (u32)(lane >> 3) * 6144 + (lane & 7) * 16, mkrs(wmod, 512u * 6144u), (u32)(64 * (b >> 5)) * 6144 + (16 + (b & 31)) * 128);
      dma1(L_LC, l16, mkrs(c, 1024u), 0);
    }
  }
  MARK(8);
  BAR();
  MARK(9);
  u32 npol;
  {
    const f32x4 s0 = *(LAS f32x4*)mp, g0 = *(LAS f32x4*)(mp + 2048), s1 = *(LAS f32x4*)(mp + 1024), g1 = *(LAS f32x4*)(mp + 3072);
    npol = *(LAS u32*)((lptr)(uintptr_t)cb + 4 * C_POL);
    const f32x2 sf[4] = {{s0[0], s0[1]}, {s0[2], s0[3]}, {s1[0], s1[1]}, {s1[2], s1[3]}}, gf[4] = {{g0[0], g0[1]}, {g0[2], g0[3]}, {g1[0], g1[1]}, {g1[2], g1[3]}};
    u32x4 o[8];
#pragma unroll
    for (int r = 0; r < 8; ++r) {
#pragma unroll
      for (int e = 0; e < 4; ++e) o[r][e] = pk2(fma2(xn[r][e], gf[e], sf[e]));
      *(LAS u32x4*)(L + (hw ^ (r * 16))) = o[r];
      if (r > 0) asm volatile("" ::"v"(o[r - 1]));
      SCHED();
    }
  }
  u32 pol13 = ((__builtin_amdgcn_readfirstlane(npol) + 31) >> 6) << 13;
  hbA ^= pol13; hbB ^= pol13; hfo ^= pol13; hso_ ^= pol13; w1s ^= pol13;
  u32 go = lo + hbB + (w < 4 ? CH_K : CH_V) * 16384 + jB14;
  PINS(pol13); PINS(hbA); PINS(hbB); PINS(hfo); PINS(hso_); PINS(w1s); PIN(go);
  BAR();
  MARK(10);

  constexpr int PD = 6;
  u32x4 o0, o1;
  u32x4 mk[2];
#define KOFF(v) (((v) & 3) + 8 * ((v) >> 2))
  lptr kp, va[9];
  u32 ovo, og;
#define MASK_SETUP() do { LANE(); const int w_ = opaque(w);                                                                                                  \
    const int bm_ = (int)((0xffffffffu >> (31 - r32)) >> (4 * hh));                                                                                          \
    _Pragma("unroll") for (int e = 0; e < 8; ++e)                                                                                                            \
      mk[e >> 2][e & 3] = ((u32)__builtin_amdgcn_sbfe(bm_, KOFF(2 * e), 1) & 0xffffu) | ((u32)__builtin_amdgcn_sbfe(bm_, KOFF(2 * e) + 1, 1) & 0xffff0000u);  \
    PIN(mk[0]); PIN(mk[1]);                                                                                                                                  \
    kp = L + L_KIMG + t16; PIN(kp);                                                                                                                          \
    const u32 lq_ = ((l16 >> 1) & 256) | (l16 & 240), up_ = (u32)__builtin_amdgcn_sbfe((int)l16, 8, 1);                                                      \
    u32 s0_ = L_VIMG + w10, s1_ = L_ONES + 16 * (w_ >> 2), s2_ = ((w_ & 4) + (jB >> 2) + CH_O) * 16384 + (jB & 3) * 2048 + (w_ & 3) * 512 + hbB;              \
    PINS(s0_); PINS(s1_); PINS(s2_);                                                                                                                         \
    const u32 vb_ = (up_ & s1_) | (~up_ & (s0_ + lq_)), vt_ = (up_ & 16u) | (~up_ & 4096u);                                                                   \
    _Pragma("unroll") for (int i = 0; i < 9; ++i) { va[i] = L + vb_ + i * vt_; PIN(va[i]); }                                                                 \
    ovo = ((l16 & 896) << 3) + jA * 128 + ((l16 & 112) ^ ((l16 >> 3) & 96)); PIN(ovo);                                                                       \
    og = s2_ + lq_ + (up_ & 32768u); PIN(og); } while (0)
  const u32x4 rsH = mkrs(ws, hsz);
  if (w < 4) {
    __builtin_amdgcn_s_setprio(2);
    f32x16 acc = {};
    u32x4 f[32];
#pragma unroll
    for (int i = 0; i < PD; ++i) { f[i] = lds128(hb[i & 3] + i * 1024); SCHED(); }
#pragma unroll
    for (int ks = 0; ks < 32; ++ks) {
      if (ks + PD < 32) f[ks + PD] = lds128(hb[(ks + PD) & 3] + (ks + PD) * 1024);
      acc = MFMA32(wf[ks], f[ks], acc);
      if (ks == 25) cnt_inc(C_V);
      SCHED();
    }
    MARK(11);
#define A2(i) (f32x2{acc[i], acc[(i) + 1]})
#pragma unroll
    for (int e = 0; e < 4; ++e) o0[e] = pk2(A2(2 * e));
    __builtin_amdgcn_raw_buffer_store_b128(o0, rW, go, 0, 16);
    SCHED();
#pragma unroll
    for (int e = 0; e < 4; ++e) o1[e] = pk2(A2(8 + 2 * e));
    __builtin_amdgcn_raw_buffer_store_b128(o1, rW, go + 1024, 0, 16);
    SCHED();
    MARK(12);
    __builtin_amdgcn_s_setprio(0);
    u32 kfl = hbB + jBf + F_K * 1024;
    PINS(kfl);
    MASK_SETUP();
    VMCNT(0);
    cnt_flag(C_K, 3, zero0, kfl, 1);
    MARK(13);
    cnt_wait(C_WQ, 4);
    __builtin_amdgcn_s_setprio(2);
    MARK(14);
    acc = f32x16{};
    {
      const lptr p = L + L_STG + wtr;
      u32x4 a[32];
      constexpr int QD = 7;
#pragma unroll
      for (int i = 0; i < QD; ++i) { a[i] = trfrag(p, i * 2048, i * 2048 + 512); SCHED(); }
#pragma unroll
      for (int ks = 0; ks < 32; ++ks) {
        acc = MFMA32(a[ks], f[ks], acc);
        if (ks + QD < 32) a[ks + QD] = trfrag(p, (ks + QD) * 2048, (ks + QD) * 2048 + 512);
        SCHED();
      }
    }
    cnt_wait(C_V, 8);
    *(LAS u32x4*)(L + L_KIMG + 32768 + lo) = o0; *(LAS u32x4*)(L + L_KIMG + 32768 + lo + 1024) = o1;
    SCHED();
#pragma unroll
    for (int e = 0; e < 4; ++e) o0[e] = pk(sc(acc[2 * e] * QSCALE), sc(acc[2 * e + 1] * QSCALE));
    *(LAS u32x4*)(L + L_Q + lo) = o0;
#pragma unroll
    for (int e = 0; e < 4; ++e) o1[e] = pk(sc(acc[8 + 2 * e] * QSCALE), sc(acc[8 + 2 * e + 1] * QSCALE));
    *(LAS u32x4*)(L + L_Q + lo + 1024) = o1;
#pragma unroll
    for (int i = 0; i < 32; ++i) UNDEF(wf[i]);
    MARK(15);
  }
  if (opaque(w) >= 4) {
    u32x4 rtf[4];
    const bool rt1 = (BG & 1) && b >= 32 && isw1, rt2 = (BG & 1) && !isw1;
    const u32 rts = isw1 ? 65536 : 2048;
#define RT_ST(n) __builtin_amdgcn_raw_buffer_store_b128(rtf[(n) & 3], rW, rto + (((n) >> 1) & 1) * rts + ((n) & 1) * 1024 + ((n) >> 2) * 8192, 0, 16)
    __builtin_amdgcn_s_setprio(3);
    dma4v(L_STG - 81920 + w10 * 20, wvoff, rsQ, (w - 4) * 160 * 3072, 8 * 3072);
    if (w < 7) {
      dma8<8 * 3072>(L_STG - 81920 + 4096 + w10 * 20, wvoff, rsQ, ((w - 4) * 160 + 32) * 3072);
      dma8<8 * 3072>(L_STG - 81920 + 12288 + w10 * 20, wvoff, rsQ, ((w - 4) * 160 + 96) * 3072);
    }
    __builtin_amdgcn_s_setprio(1);
    LANE();
    const int P = opaque(b) - 32, j2 = (P - 112) / 14, ks2 = 4 * (P - 112 - 14 * j2) + (w & 3), ks1 = 4 * (P & 3) + (w & 3);
    const u32 rto = l16 + (isw1 ? WS_W1T + (P >> 2) * 131072 + (ks1 >> 2) * 16384 + (ks1 & 3) * 2048 : WS_W2T + (7 * j2 + (ks2 >> 3)) * 65536 + ((ks2 & 7) >> 1) * 16384 + (ks2 & 1) * 4096);
    if (w < 7) VMCNT(20); else VMCNT(4);
    {
      const lptr p = L + L_RT + (w - 4) * 4096 + ((isw1 ? 8 : 4) * G + q4) * 128 + 8 * pp;
#pragma unroll
      for (int f = 0; f < 4; ++f) { const u32x2 a = ldstr(p + 32 * f), bq = ldstr(p + (isw1 ? 4 * 128 : 16 * 128) + 32 * f); rtf[f] = u32x4{a.x, a.y, bq.x, bq.y}; }
    }
    if (rt1) {
#pragma unroll
      for (int i = 0; i < 8; ++i) RT_ST(i);
    }
    if (w == 7) {
      touch((lane < 8 ? hbB + jB * 128 : hbA + jA * 128) + (lane & 7) * 1024, rsW, CH_FL * 16384);
      dma1(L_B1, l16, mkrs(b1, 3584u), 448 * jB);
      dma1(L_B2, l16, mkrs(b2, 1024u), 128 * jA);
      const float sl = silu_bf(lo16(*(LAS u16*)(L + L_LC + (64 * (b >> 5) + lane) * 2)));
      *(LAS u16*)(L + L_LC + 1024 + lane * 2) = (u16)(asu(sl) >> 16);
      const lptr p = L + L_LM + (8 * G + q4) * 128 + 8 * pp;
      f32x4 a[4];
#pragma unroll
      for (int k = 0; k < 2; ++k) {
        const u32x4 sf = lds128(L + L_LC + 1024 + k * 64 + G * 16);
#pragma unroll
        for (int nt = 0; nt < 4; ++nt) a[nt] = MFMA16(sf, trfrag(p, k * 4096 + nt * 32, k * 4096 + 512 + nt * 32), k ? a[nt] : f32x4{});
      }
      const float sm = (G & 2) ? ((G & 1) ? a[3][0] : a[2][0]) : ((G & 1) ? a[1][0] : a[0][0]);
      __builtin_amdgcn_raw_buffer_store_b64(u32x2{((u32)tag << 24) | ((asu(sm) + 0x80u) >> 8), (u32)(tag >> 8)}, rW, lane * 8, WS_MODP + ((b & 31) * 8 + (b >> 5)) * 512, 16);
    }
    MARK(11);
    f32x16 acc = {};
    {
      u32x4 f[32];
#pragma unroll
      for (int i = 0; i < PD; ++i) { f[i] = lds128(hb[i & 3] + i * 1024); SCHED(); }
      cnt_wait(C_V, 4);
      MARK(18);
#pragma unroll
      for (int ks = 0; ks < 32; ++ks) {
        if (ks + PD < 32) f[ks + PD] = lds128(hb[(ks + PD) & 3] + (ks + PD) * 1024);
        acc = MFMA32(f[ks], wf[ks], acc);
        if (ks == 18) VMCNT(0);
        if (ks == 22) cnt_inc(C_WQ);
        if (ks >= 19 && ks % 4 == 3 && rt2) { RT_ST((ks - 19) / 2); RT_ST((ks - 19) / 2 + 1); }
        SCHED();
      }
    }
#pragma unroll
    for (int e = 0; e < 4; ++e) o0[e] = pk2(A2(2 * e));
    __builtin_amdgcn_raw_buffer_store_b128(o0, rW, go, 0, 16);
    SCHED();
#pragma unroll
    for (int e = 0; e < 4; ++e) o1[e] = pk2(A2(8 + 2 * e));
    __builtin_amdgcn_raw_buffer_store_b128(o1, rW, go + 512, 0, 16);
    SCHED();
    __builtin_amdgcn_s_setprio(0);
    cnt_inc(C_V);
    MARK(12);
    MASK_SETUP();
    u32 vfo = lane ? WS_WFLAG + b * 128 : hbB + jBf + F_V * 1024;
    PIN(vfo);
    const u64 pk0 = peek(rsW, zero0, hfo, 1);
    cnt_wait(C_V, 8);
    VMCNT(0);
    cnt_flag(C_V, 11, vfo, 0, 3);
    MARK(13);
    poll_rest<false>(rsW, zero0, hfo, tag, 1, pk0);
    MARK(14);
    dma4_sc1(L_KIMG - 32768 + w10 * 8, l16, rsH, hso_);
    dma4_sc1(L_KIMG - 32768 + 4096 + w10 * 8, l16, rsH, hso_ + 4096);
    MARK(15);
  }
  u32 wfl, gi8;
  if (opaque(w) == 7) {
    LANE();
    wfl = (32 + lane + (lane < 16 ? 4 * ((7 * jB) >> 1) : 96 + 14 * jA)) * 128;
    gi8 = ((hh ? 2560 : 1024) + 64 * jA + 2 * r32) * 8;
    PIN(wfl); PIN(gi8);
  }
  BAR();
  MARK(16);

  u32x4 w1f[2][16], xres, ga, gc;
#define W1_LD1(n) w1f[(n) & 1][(n) >> 1] = __builtin_amdgcn_raw_buffer_load_b128(rW, l16 + ((n) & 3) * 1024, w1s + ((n) >> 3) * 16384 + ((n) & 4) * 1024, 16)
#define W1N(n) (n)
  {
    const f32x16 zero = {};
#define EXP2(v) __builtin_amdgcn_exp2f(v)
#define PK4(s, o) u32x4{pk(EXP2(s[o]), EXP2(s[o + 1])), pk(EXP2(s[o + 2]), EXP2(s[o + 3])), pk(EXP2(s[o + 4]), EXP2(s[o + 5])), pk(EXP2(s[o + 6]), EXP2(s[o + 7]))}
#define MX3(a, b, c) __builtin_fmaxf(__builtin_fmaxf(a, b), c)
    const u32x4 qf = lds128(kp + L_Q);
    f32x16 acc;
    {
      u32x4 kf[9], vf[18], p[18];
      kf[0] = lds128(kp + 8 * 4096);
      __builtin_amdgcn_s_setprio(3);
      f32x16 sA = MFMA32(kf[0], qf, zero), sB;
      u64 pk0;
      if (opaque(w) < 4) { cnt_inc(C_KH); pk0 = peek(rsW, zero0, hfo, 1); }
      if (opaque(w) >= 4) {
        *(LAS u32x4*)(L + L_VIMG + 32768 + lo) = o0; *(LAS u32x4*)(L + L_VIMG + 32768 + lo + 512) = o1;
        cnt_inc(C_VH);
        VMCNT(0);
        cnt_inc(C_KH);
      }
      u64 pk1;
      if (opaque(w) == 7) pk1 = peek(rsW, wfl, WS_WFLAG, 0x3fffffffull);
      SCHED();
      float m = MX3(MX3(MX3(sA[0], sA[1], sA[2]), MX3(sA[3], sA[4], sA[5]), MX3(sA[6], sA[7], sA[8])), MX3(sA[9], sA[10], sA[11]), MX3(MX3(sA[12], sA[13], sA[14]), sA[15], sA[0]));
      { auto r = __builtin_amdgcn_permlane32_swap(asu(m), asu(m), false, false); m = MX3(asf(r[0]), asf(r[1]), asf(r[0])); }
      f32x16 nm;
#pragma unroll
      for (int v = 0; v < 16; ++v) nm[v] = -m;
      PIN(nm);
      sA = MFMA32(kf[0], qf, nm);
      SCHED();
      MARK(20);
      const u32x4 rsO = mkrs(wo, 512u * 1024u);
      cnt_wait(C_KH, 8);
      MARK(21);
      float e0, e1, e2, e3;
#define VRD(n) vf[n] = lds128(va[(n) < 2 ? 8 : ((n) - 2) >> 1] + ((n) & 1) * 512)
#define PV(n) do { acc = MFMA32(vf[n], p[n], (n) ? acc : zero); if ((n) + 3 < 18) VRD((n) + 3); } while (0)
#define MSK(v, qi, e) ((qi) < 4 ? (v) & mk[(qi) >> 1][2 * ((qi) & 1) + (e)] : (qi) < 8 ? (v) & ~mk[((qi) >> 1) & 1][2 * ((qi) & 1) + (e)] : (v))
#define CV(qi) do { if ((qi) >= 0) { p[(qi) >> 1][2 * ((qi) & 1)] = MSK(pk(e0, e1), qi, 0); p[(qi) >> 1][2 * ((qi) & 1) + 1] = MSK(pk(e2, e3), qi, 1); PIN(p[(qi) >> 1]); } } while (0)
#define QT(i, S, qq, MF) do { const float n0_ = EXP2(S[4 * (qq)]), n1_ = EXP2(S[4 * (qq) + 1]); float m0_ = n0_, m1_ = n1_; PIN(m0_); PIN(m1_); SCHED();               \
                              CV(4 * (i) + (qq) - 1); SCHED(); e0 = m0_; e1 = m1_; e2 = EXP2(S[4 * (qq) + 2]); e3 = EXP2(S[4 * (qq) + 3]); PIN(e2); PIN(e3); SCHED(); MF; \
                              if ((BG & 8) && 4 * (i) + (qq) >= WO0 && 4 * (i) + (qq) < WO0 + 8) dma1(L_WO + w10 * 8 + (4 * (i) + (qq) - WO0) * 1024, ovo, rsO, w10 * 64 + 8 * (4 * (i) + (qq) - WO0) * 1024); \
                              if (4 * (i) + (qq) >= WO0 + 8 && 4 * (i) + (qq) < WO0 + 8 + 2 * NAT) W1_LD1((4 * (i) + (qq) - WO0 - 8) & 31); SCHED(); } while (0)
#define TILE(i, S, M0, M1, M2, M3) do { QT(i, S, 0, M0); QT(i, S, 1, M1); QT(i, S, 2, M2); QT(i, S, 3, M3); } while (0)
      static_assert(WO0 >= 4 && WO0 + 8 >= 16 && WO0 + 8 + 2 * NAT <= 32, "W1 loads start behind the flag check");
#pragma unroll
      for (int i = 1; i < 9; ++i) { kf[i] = lds128(kp + (i - 1) * 4096); SCHED(); }
      TILE(0, sA, , sB = MFMA32(kf[1], qf, nm), , sA = MFMA32(kf[2], qf, nm));
      if (opaque(w) < 4) {
        poll_rest(rsW, zero0, hfo, tag, 1, pk0);
        MARK(17);
        dma4_sc1(L_VIMG + w10 * 8, l16, rsH, hso_);
        dma4_sc1(L_VIMG + 4096 + w10 * 8, l16, rsH, hso_ + 4096);
      }
      TILE(1, sB, , , , sB = MFMA32(kf[3], qf, nm));
      TILE(2, sA, , , , sA = MFMA32(kf[4], qf, nm));
      __builtin_amdgcn_s_setprio(2);
      if (w < 4) { VMCNT((BG & 8) && WO0 < 12 ? 12 - WO0 : 0); cnt_inc(C_VH); }
      if (opaque(w) == 7) { poll_rest(rsW, wfl, WS_WFLAG, tag, 0x3fffffffull, pk1); cnt_inc(C_VH); }
      if (opaque(w) == 6 && b >= 224) {
        LANE();
        u32x4 pg[4];
#pragma clang loop unroll(disable)
        for (int s = 0; s < SPIN_CAP; ++s) {
          asm volatile("" ::: "memory");
#pragma unroll
          for (int q = 0; q < 4; ++q) pg[q] = __builtin_amdgcn_raw_buffer_load_b128(rW, q * 1024 + l16, WS_MODP + (b & 31) * 4096, 16);
          if (__all((gran_bad<24>(pg[0], tag) | gran_bad<24>(pg[1], tag) | gran_bad<24>(pg[2], tag) | gran_bad<24>(pg[3], tag)) == 0)) break;
        }
        VMCNT0();
        const float c0 = (asf(pg[0].x << 8) + asf(pg[1].x << 8)) + (asf(pg[2].x << 8) + asf(pg[3].x << 8));
        const float c1 = (asf(pg[0].z << 8) + asf(pg[1].z << 8)) + (asf(pg[2].z << 8) + asf(pg[3].z << 8));
        const float sm = add32(c0, c1);
        const u64 gr = (tag << 16) | (u64)(pk(sm, sm) & 0xffffu);
        __builtin_amdgcn_raw_buffer_store_b64(__builtin_bit_cast(u32x2, gr), rW, (2 * r32 + hh) * 8, WS_MODG + (16 + (b & 31)) * 512, 16);
      }
      TILE(3, sB, , , , sB = MFMA32(kf[5], qf, nm));
      MARK(22);
      cnt_wait(C_VH, 9);
      MARK(23);
      VRD(0); VRD(1); VRD(2);
      SCHED();
      TILE(4, sA, , PV(0), PV(1), sA = MFMA32(kf[6], qf, nm));
      __builtin_amdgcn_s_setprio(1);
      TILE(5, sB, PV(2), PV(3), PV(4), sB = MFMA32(kf[7], qf, nm));
      TILE(6, sA, PV(5), PV(6), PV(7), sA = MFMA32(kf[8], qf, nm));
      __builtin_amdgcn_s_setprio(0);
      TILE(7, sB, PV(8), PV(9), PV(10), PV(11));
      asm volatile("buffer_load_dwordx4 %0, %2, %4, %5 offen sc1\n\ts_cmp_eq_u32 %7, 7\n\ts_cbranch_scc0 1f\n\tbuffer_load_dwordx4 %1, %3, %4, %6 offen sc1\n1:"
                   : "=&v"(ga), "=&v"(gc) : "v"(t16), "v"(gi8), "s"(rsW), "s"(WS_MODG + 12288), "s"(WS_MODG), "s"(w) : "memory", "scc");
      SCHED();
      TILE(8, sA, PV(12), PV(13), PV(14), PV(15));
      PV(16);
      CV(35);
      PV(17);
    }
    if (__any(!(acc[8] < 1.0995116e12f && acc[8] > 9.094947e-13f))) {
      LANE();
      const u32 vbase = r32 < 16 ? L_VIMG + w10 + hh * 256 + r32 * 16 : L_ONES + 16 * qh, vts = r32 < 16 ? 4096u : 16u;
      const int istart = 8 - 2 * rbB - qh;
      float m = -INFINITY;
#pragma clang loop unroll(disable)
      for (int pass = 0; pass < 2; ++pass) {
        acc = zero;
#pragma clang loop unroll(disable)
        for (int i = istart > 0 ? istart : 0; i < 9; ++i) {
          f32x16 s = MFMA32(lds128(kp + i * 4096), qf, zero);
          int dq = 256 - 32 * i + r32 - 4 * hh;
          PIN(dq);
#pragma unroll
          for (int v = 0; v < 16; ++v) s[v] = (u32)(dq - KOFF(v)) < 256u ? s[v] : -INFINITY;
          if (pass == 0) {
#pragma unroll
            for (int v = 0; v < 16; ++v) m = __builtin_fmaxf(m, s[v]);
          } else {
#pragma unroll
            for (int v = 0; v < 16; ++v) s[v] -= m;
            acc = MFMA32(lds128(L + vbase + i * vts), PK4(s, 0), acc);
            acc = MFMA32(lds128(L + vbase + i * vts + 512), PK4(s, 8), acc);
          }
        }
        auto r = __builtin_amdgcn_permlane32_swap(asu(m), asu(m), false, false);
        m = __builtin_fmaxf(asf(r[0]), asf(r[1]));
      }
    }
    const float rl = __builtin_amdgcn_rcpf(acc[8]);
    u32x4 o;
#pragma unroll
    for (int e = 0; e < 4; ++e) o[e] = pk(sc(acc[2 * e] * rl), sc(acc[2 * e + 1] * rl));
    __builtin_amdgcn_raw_buffer_store_b128(o, rW, og, 0, 16);
    SCHED();
  }
  MARK(24);
  u32 ofl = hbB + jBf + F_O * 1024, opl = fl_off(hbA, F_O, 4 * kh), l128 = l16 * 8, oso = hbA + (CH_O + w) * 16384;
  PINS(ofl); PINS(opl); PIN(l128); PINS(oso);
  lptr wb[4];
  u32 xo;
  {
    LANE();
    xo = (rbA * 64 + mt * 16 + tok) * 1024 + (64 * jA + 32 * kh + 16 * (G & 1) + 8 * (G >> 1)) * 2;
    PIN(xo);
    const int sw = 2 * (G & 1) + (q4 >> 1);
#pragma unroll
    for (int nt = 0; nt < 4; ++nt) { wb[nt] = L + L_WO + (256 * kh + 16 * (G >> 1) + 4 * (G & 1) + q4) * 128 + (pp >> 1) * 16 + 8 * (pp & 1) + (nt ^ sw) * 32; PIN(wb[nt]); }
  }
  asm volatile("s_waitcnt vmcnt(1)" : "+v"(ga), "+v"(gc)::"memory");
#pragma clang loop unroll(disable)
  for (int s = 0; s < SPIN_CAP; ++s) {
    if (__all(gran_bad<16>(ga, tag) == 0)) break;
    asm volatile("buffer_load_dwordx4 %0, %1, %2, %3 offen sc1\n\ts_waitcnt vmcnt(0)" : "=&v"(ga) : "v"(t16), "s"(rsW), "s"(WS_MODG + 12288) : "memory");
  }
  *(LAS u32*)(L + L_MOD + 3072 + (t16 >> 2)) = w >= 4 ? pk(1.f + lo16(ga.x), 1.f + lo16(ga.z)) : (ga.x & 0xffffu) | (ga.z << 16);
  if (opaque(w) == 7) {
#pragma clang loop unroll(disable)
    for (int s = 0; s < SPIN_CAP; ++s) {
      if (__all(gran_bad<16>(gc, tag) == 0)) break;
      asm volatile("buffer_load_dwordx4 %0, %1, %2, %3 offen sc1\n\ts_waitcnt vmcnt(0)" : "=&v"(gc) : "v"(gi8), "s"(rsW), "s"(WS_MODG) : "memory");
    }
    *(LAS u32*)(L + L_MOD + (gi8 >> 2)) = (gc.x & 0xffffu) | (gc.z << 16);
  }
  VMCNT0();
  MARK(25);
  BAR();
  if (w == 7) set_flag(rsW, zero0, ofl, tag, 1);
  MARK(26);

  u32x4 x1p;
  {
    if (w < 7) {
#pragma unroll
      for (int n = 2 * NAT; n < 2 * NPO; ++n) { W1_LD1(n); SCHED(); }
    }
    xres = __builtin_amdgcn_raw_buffer_load_b128(__builtin_amdgcn_make_buffer_rsrc((void*)x, 0, 2048 * 1024, 0x00020000), xo, 0, 0);
    SCHED();
    u32x4 a[32];
#pragma unroll
    for (int i = 0; i < OD; ++i) { a[i] = trfrag(wb[i & 3], (i >> 2) * 4096, (i >> 2) * 4096 + 1024); SCHED(); }
    const u64 pk0 = peek(rsW, l128, opl, 15);
    LANE();
    const int col0 = 64 * jA + 32 * kh + 16 * (G & 1) + 8 * (G >> 1);
    const u32x4 gav = lds128(L + L_MOD + 2048 + col0 * 2), g2v = lds128(L + L_MOD + 4096 + col0 * 2);
    lptr xw = L + L_XCH + w * 2048 + l16, xr_ = L + L_XCH + (w ^ 1) * 2048 + l16;
    u32 gx = ((2 * (G & 1) + (G >> 1)) * 16 + tok) * 16 + hbA + (CH_X + 2 * mt + (jA >> 2)) * 16384 + (2 * (jA & 3) + kh) * 1024;
    u32 gs = lane < 32 ? lane * 4 + hbA + CH_ST * 16384 + (mt * 16 + 2 * jA + kh) * 128 : 0xfffffff0u;
    PIN(xw); PIN(xr_); PIN(gx); PIN(gs);
    poll_rest(rsW, l128, opl, tag, 15, pk0);
    MARK(27);
    u32x4 of[8];
#pragma unroll
    for (int k = 0; k < 8; ++k) { of[k] = __builtin_amdgcn_raw_buffer_load_b128(rW, (k & 3) * 1024 + l16, oso + (k & 4) * 1024, 16); SCHED(); }
    MARK(28);
    f32x4 acc[4] = {};
    f32x2 gaf[4], g2f[4], xf[4];
#pragma unroll
    for (int i = 0; i < 32; ++i) {
      acc[i & 3] = MFMA16(a[i], of[i >> 2], acc[i & 3]);
      if (i + OD < 32) a[i + OD] = trfrag(wb[(i + OD) & 3], ((i + OD) >> 2) * 4096, ((i + OD) >> 2) * 4096 + 1024);
      if (i >= 16 && i < 20) { gaf[i & 3] = up2(gav[i & 3]); PIN(gaf[i & 3]); }
      if (i >= 20 && i < 24) { g2f[i & 3] = up2(g2v[i & 3]); PIN(g2f[i & 3]); }
      if (i >= 24 && i < 28) { xf[i & 3] = up2(xres[i & 3]); PIN(xf[i & 3]); }
      SCHED();
    }
    f32x4 t0, t1;
    if (kh) { *(LAS f32x4*)xw = acc[0]; *(LAS f32x4*)(xw + 1024) = acc[1]; t0 = acc[2]; t1 = acc[3]; }
    else { *(LAS f32x4*)xw = acc[2]; *(LAS f32x4*)(xw + 1024) = acc[3]; t0 = acc[0]; t1 = acc[1]; }
    PIN(t0); PIN(t1);
    MARK(29);
    BAR();
    MARK(30);
    t0 += *(LAS f32x4*)xr_;
    t1 += *(LAS f32x4*)(xr_ + 1024);
    float y[8];
#pragma unroll
    for (int r = 0; r < 4; ++r) { auto s = __builtin_amdgcn_permlane16_swap(asu(t0[r]), asu(t1[r]), false, false); y[r] = asf(s[0]); y[4 + r] = asf(s[1]); }
    u32x4 xg;
    f32x2 xv[4];
#pragma unroll
    for (int e = 0; e < 4; ++e) { xv[e] = fma2(gaf[e], f32x2{y[2 * e], y[2 * e + 1]}, xf[e]); xg[e] = pk2(xv[e] * g2f[e]); }
    __builtin_amdgcn_raw_buffer_store_b128(xg, rW, gx, 0, 16);
    SCHED();
    const f32x2 s1 = (xv[0] + xv[1]) + (xv[2] + xv[3]), s2 = fma2(xv[3], xv[3], fma2(xv[2], xv[2], fma2(xv[1], xv[1], xv[0] * xv[0])));
    float z = add16(s1.x + s1.y, s2.x + s2.y);
    z = add32(z, z);
    __builtin_amdgcn_raw_buffer_store_b32(asu(z), rW, gs, 0, 16);
    SCHED();
#pragma unroll
    for (int e = 0; e < 4; ++e) x1p[e] = pk2(xv[e]);
    PIN(x1p);
  }
  MARK(31);
  u32 xfl = fl_off(hbA, F_X, jA), xpl = fl_off(hbB, F_X, 0);
  const u32 w2h = ((rbA >> 4) * 8192) ^ pol13;
  PINS(xfl); PINS(xpl);
  f32x2 gf[4], cf[4];
  lptr p;
  GAS u32x4* op;
#define FINAL_SETUP() do { PIN(l16); const int G_ = l16 >> 8, tok_ = (l16 >> 4) & 15, cl_ = 32 * kh + 16 * (G_ & 1) + 8 * (G_ >> 1), col_ = 64 * jA + cl_;       \
    const u32x4 gb = lds128(L + L_MOD + 5120 + col_ * 2), b2v = lds128(L + L_B2 + cl_ * 2);                                                                      \
    _Pragma("unroll") for (int e = 0; e < 4; ++e) { gf[e] = up2(gb[e]); cf[e] = f32x2{sc(__builtin_fmaf(gf[e].x, lo16(b2v[e]), lo16(x1p[e]))), sc(__builtin_fmaf(gf[e].y, hi16(b2v[e]), hi16(x1p[e])))}; PIN(gf[e]); PIN(cf[e]); }                 \
    p = L + (mt < 2 ? L_W2 : L_PB) + (mt & 1) * 4096 + (2 * kh + (G_ & 1)) * 1024 + ((2 * (G_ >> 1)) * 16 + tok_) * 16;                                          \
    op = (GAS u32x4*)(out + (size_t)(rbA * 64 + mt * 16 + tok_) * 512 + col_); PIN(p); PIN(op); } while (0)
  if (w < 7) {
    LANE();
    float uu[8], vv[8];
    u64 pk0;
    {
      f32x4 a0 = {}, a1 = {};
      const lptr p = L + L_MOD + ((tok & 1) ? 3072 : 4096) + G * 16;
      u32x4 f[16];
#pragma unroll
      for (int i = 0; i < 6; ++i) { f[i] = lds128(p + i * 64); SCHED(); }
#pragma unroll
      for (int ks = 0; ks < 16; ++ks) {
        if (ks == 8) {
          VMCNT(0);
          asm volatile("s_barrier" ::: "memory");
          MARK(32);
#pragma unroll
          for (int n = 2 * NPO; n < 32; ++n) { W1_LD1(n); SCHED(); }
          pk0 = peek(rsW, zero0, xpl + w * 128, 1);
        }
        if (ks + 6 < 16) f[ks + 6] = lds128(p + (ks + 6) * 64);
        a0 = MFMA16(w1f[0][ks], f[ks], a0); a1 = MFMA16(w1f[1][ks], f[ks], a1);
        SCHED();
      }
      const u32x2 bb0 = *(LAS u32x2*)(L + L_B1 + (32 * w + 4 * G) * 2), bb1 = *(LAS u32x2*)(L + L_B1 + (32 * w + 4 * G + 16) * 2);
      poll_rest(rsW, zero0, xpl + w * 128, tag, 1, pk0);
      MARK(33);
#pragma unroll
      for (int m = 0; m < 4; ++m) dma2_sc1(L_X1 + (m * 16 + 2 * w) * 1024, l16, rsW, hbB + (CH_X + 2 * m + (w >> 2)) * 16384 + 2 * (w & 3) * 1024);
#pragma unroll
      for (int r = 0; r < 4; ++r) {
        uu[r] = dpp<0xA0>(a0[r]); uu[4 + r] = dpp<0xA0>(a1[r]);
        vv[r] = dpp<0xF5>(a0[r]) + ((r & 1) ? hi16(bb0[r >> 1]) : lo16(bb0[r >> 1]));
        vv[4 + r] = dpp<0xF5>(a1[r]) + ((r & 1) ? hi16(bb1[r >> 1]) : lo16(bb1[r >> 1]));
      }
#pragma unroll
      for (int r = 0; r < 8; ++r) { PIN(uu[r]); PIN(vv[r]); }
    }
    u32 w2s = WS_W2T + (7 * jA + w) * 65536 + (w2h ^ (((7 * jA + w) >> 5) * 8192)), sl2 = L_W2 + w * 12288;
    u32 hst = hbB + (CH_H + ((7 * jB + w) >> 3)) * 16384 + ((7 * jB + w) & 7) * 1024 + l16;
    u32 hfa = hbB + jBf + F_HA * 1024, hso = hbA + (CH_H + w) * 16384;
    lptr xp = L + L_X1 + l16, stp = L + L_STAT + tok * 8;
    PINS(w2s); PINS(sl2); PIN(hst); PINS(hfa); PINS(hso); PIN(xp); PIN(stp);
    u32x4 w2f[8][4];
#define W2_LD(k, nt) w2f[k][nt] = __builtin_amdgcn_raw_buffer_load_b128(rW, l16 + (nt) * 1024, w2s + ((k) >> 1) * 16384 + ((k) & 1) * 4096, 16)
#define W2_DMA(dst, base, k) dma4_sc1(dst, l16, rsW, (base) + ((k) >> 1) * 16384 + ((k) & 1) * 4096)
#define W2N(n) (n)
    VMCNT(4);
    BAR();
    MARK(34);
    auto epi = [&](f32x4 (&acc)[2][2], const f32x2 (&st)[2], float (&tt)[2][8], int half) {
#pragma unroll
      for (int m = 0; m < 2; ++m) {
        float v[8];
        if (half == 0) {
#pragma unroll
          for (int e = 0; e < 8; ++e) tt[m][e] = sc(__builtin_fmaf(st[m].y, uu[e], vv[e]));
        }
#pragma unroll
        for (int e = 0; e < 8; ++e) v[e] = sc(__builtin_fmaf(st[m].x, acc[m][e >> 2][e & 3], tt[m][e]));
#pragma unroll
        for (int e = 0; e < 8; ++e) v[e] = sc(v[e] > 0.f ? v[e] : 0.f);
#pragma unroll
        for (int e = 0; e < 8; ++e) v[e] = sc(v[e] * v[e]);
        __builtin_amdgcn_raw_buffer_store_b128(u32x4{pk(v[0], v[1]), pk(v[2], v[3]), pk(v[4], v[5]), pk(v[6], v[7])}, rW, hst + 7 * (2 * half + m) * 16384, 0, 16);
        SCHED();
      }
    };
    constexpr int XD = 3;
    if (w >= 4) {
#pragma unroll
      for (int i = 0; i < 8; ++i) { W2_LD(i >> 2, i & 3); SCHED(); }
      W2_DMA(sl2, w2s, 2); W2_DMA(sl2 + 4096, w2s, 3);
      VMCNT(W2N(16));
      cnt_inc(C_XB);
    }
    {
      __builtin_amdgcn_s_setprio(1);
      f32x4 acc[2][2] = {};
      u32x4 f[16][2];
      f32x2 st[2];
#pragma unroll
      for (int i = 0; i < XD; ++i) { f[i][0] = lds128(xp + i * 1024); SCHED(); f[i][1] = lds128(xp + 16384 + i * 1024); SCHED(); }
#pragma unroll
      for (int ks = 0; ks < 16; ++ks) {
#pragma unroll
        for (int m = 0; m < 2; ++m) {
          if (ks + XD < 16) f[ks + XD][m] = lds128(xp + m * 16384 + (ks + XD) * 1024);
          acc[m][0] = MFMA16(w1f[0][ks], f[ks][m], acc[m][0]);
          acc[m][1] = MFMA16(w1f[1][ks], f[ks][m], acc[m][1]);
          SCHED();
        }
      }
      MARK(35);
      cnt_inc(C_WF);
      cnt_wait(C_STAT, 1);
      st[0] = *(LAS f32x2*)stp; st[1] = *(LAS f32x2*)(stp + 128);
      if (w < 4) { VMCNT(0); cnt_inc(C_XB); }
      MARK(36);
      float tt[2][8];
      epi(acc, st, tt, 0);
      __builtin_amdgcn_s_setprio(0);
    }
    MARK(37);
    if (w < 4) {
#pragma unroll
      for (int i = 0; i < 8; ++i) { W2_LD(i >> 2, i & 3); SCHED(); }
    } else {
      cnt_wait(C_WF, 7);
      W2_DMA(L_X1 + (w - 4) * 12288, w2s, 5); W2_DMA(L_X1 + (w - 4) * 12288 + 4096, w2s, 6); W2_DMA(w < 6 ? L_X1 + (w - 4) * 12288 + 8192 : L_W2 + 86016, w2s, 7);
      VMCNT(W2N(12));
      cnt_flag(C_HA, 6, zero0, hfa, 1);
    }
    cnt_wait(C_XB, 8);
    MARK(38);
    {
      f32x4 acc[2][2] = {};
      u32x4 f[16][2];
      f32x2 st[2];
      float tt[2][8];
#pragma unroll
      for (int i = 0; i < XD; ++i) { f[i][0] = lds128(xp + 32768 + i * 1024); SCHED(); f[i][1] = lds128(xp + 49152 + i * 1024); SCHED(); }
#pragma unroll
      for (int ks = 0; ks < 16; ++ks) {
#pragma unroll
        for (int m = 0; m < 2; ++m) {
          if (ks + XD < 16) f[ks + XD][m] = lds128(xp + 32768 + m * 16384 + (ks + XD) * 1024);
          if (ks == 2) st[m] = *(LAS f32x2*)(stp + 256 + m * 128);
          if (ks >= 6 && ks < 14) { tt[m][ks - 6] = __builtin_fmaf(st[m].y, uu[ks - 6], vv[ks - 6]); PIN(tt[m][ks - 6]); }
          acc[m][0] = MFMA16(w1f[0][ks], f[ks][m], acc[m][0]);
          acc[m][1] = MFMA16(w1f[1][ks], f[ks][m], acc[m][1]);
          SCHED();
        }
        if (ks == 10 && w < 4) { VMCNT(W2N(8)); cnt_flag(C_HA, 6, zero0, hfa, 1); SCHED(); }
      }
      MARK(39);
      epi(acc, st, tt, 1);
    }
    MARK(40);
    if (opaque(w) >= 4) {
      VMCNT(2);
#pragma unroll
      for (int nt = 0; nt < 4; ++nt) {
        w2f[5][nt] = lds128(L + L_X1 + (w - 4) * 12288 + nt * 1024 + l16);
        w2f[6][nt] = lds128(L + L_X1 + (w - 4) * 12288 + 4096 + nt * 1024 + l16);
        w2f[7][nt] = lds128(L + (w < 6 ? L_X1 + (w - 4) * 12288 + 8192 : L_W2 + 86016) + nt * 1024 + l16);
      }
      VMCNT(0);
    }
    if (opaque(w) < 4) {
#pragma unroll
      for (int i = 0; i < 12; ++i) { W2_LD(5 + (i >> 2), i & 3); SCHED(); }
      VMCNT(W2N(12));
    }
    cnt_flag(C_HB, 6, zero0, hfa + 1024, 1);
    MARK(42);
    cnt_wait(C_RA, 1);
    MARK(41);
    u32x4 hf[4][8];
#define H_LD(m, k) hf[m][k] = __builtin_amdgcn_raw_buffer_load_b128(rW, ((k) & 3) * 1024 + l16, hso + 7 * (m) * 16384 + ((k) & 4) * 1024, 16)
#pragma unroll
    for (int i = 0; i < 12; ++i) w2f[2 + (i >> 2)][i & 3] = lds128(L + sl2 + i * 1024 + l16);
    SCHED();
#pragma unroll
    for (int k = 0; k < 16; ++k) { H_LD(k >> 3, k & 7); SCHED(); }
    MARK(43);
#pragma unroll
    for (int m = 0; m < 4; ++m) {
      f32x4 acc[4] = {};
#pragma unroll
      for (int k = 0; k < 8; ++k) {
#pragma unroll
        for (int nt = 0; nt < 4; ++nt) acc[nt] = MFMA16(w2f[k][nt], hf[m][k], acc[nt]);
        SCHED();
      }
      MARK(44 + 2 * m);
      if (m == 1) {
        cnt_wait(C_RB, 1);
        MARK(47);
#pragma unroll
        for (int k = 0; k < 16; ++k) { H_LD(2 + (k >> 3), k & 7); SCHED(); }
        FINAL_SETUP();
      }
      const lptr pd = L + (m < 2 ? sl2 : L_PB + w * 8192) + (m & 1) * 4096 + l16;
#pragma unroll
      for (int nt = 0; nt < 4; ++nt) *(LAS f32x4*)(pd + nt * 1024) = acc[nt];
      SCHED();
    }
  } else {
    LANE();
    VMCNT0();
    asm volatile("s_barrier" ::: "memory");
    set_flag(rsW, zero0, xfl, tag, 1);
    MARK(32);
    poll(rsW, l128, xpl, tag, 255);
    MARK(33);
#pragma unroll
    for (int m = 0; m < 2; ++m) dma2_sc1(L_X1 + (m * 16 + 14) * 1024, l16, rsW, hbB + (CH_X + 2 * m + 1) * 16384 + 6144);
    u32x4 sv[8];
#pragma unroll
    for (int k = 0; k < 8; ++k) { sv[k] = __builtin_amdgcn_raw_buffer_load_b128(rW, (k & 3) * 1024 + l16, hbB + CH_ST * 16384 + (k & 4) * 1024, 16); SCHED(); }
    VMCNT(8);
    BAR();
    MARK(34);
    VMCNT0();
#pragma unroll
    for (int m = 2; m < 4; ++m) dma2_sc1(L_X1 + (m * 16 + 14) * 1024, l16, rsW, hbB + (CH_X + 2 * m + 1) * 16384 + 6144);
    __builtin_amdgcn_s_setprio(3);
    {
      float z[4];
#pragma unroll
      for (int m = 0; m < 4; ++m) {
        float a[4];
#pragma unroll
        for (int e = 0; e < 4; ++e) a[e] = sc(asf(sv[2 * m][e]) + asf(sv[2 * m + 1][e]));
        z[m] = add32(add16(a[0], a[1]), add16(a[2], a[3]));
        z[m] += dpp<DPP_ROR8>(z[m]);
      }
      const float za = (q4 & 2) ? z[2] : z[0], zb = (q4 & 2) ? z[3] : z[1];
      const float oa = dpp<DPP_ROR4>(z[0]), ob = dpp<DPP_ROR4>(z[1]), oc = dpp<DPP_ROR4>(z[2]), od = dpp<DPP_ROR4>(z[3]);
      const float s1 = (q4 & 1) ? ((q4 & 2) ? od : ob) : za, s2 = (q4 & 1) ? zb : ((q4 & 2) ? oc : oa);
      const float mean = s1 * (1.f / 512.f);
      const float var = __builtin_fmaxf(__builtin_fmaf(s2, 1.f / 512.f, -mean * mean), 0.f);
      const float rs = __builtin_amdgcn_rsqf(var + 1e-5f);
      *(LAS f32x2*)(L + L_STAT + (q4 * 16 + 4 * pp + G) * 8) = f32x2{rs, -mean * rs};
    }
    cnt_inc(C_STAT);
    __builtin_amdgcn_s_setprio(0);
    VMCNT(0);
    cnt_inc(C_XB);
    MARK(36);
#pragma clang loop unroll(disable)
    for (int v = 0; v < 7; ++v) {
      const u32 so = WS_W2T + (7 * jA + v) * 65536 + (w2h ^ (((7 * jA + v) >> 5) * 8192));
      if (v < 4) { W2_DMA(L_W2 + v * 12288, so, 2); W2_DMA(L_W2 + v * 12288 + 4096, so, 3); }
      W2_DMA(L_W2 + v * 12288 + 8192, so, 4);
    }
    MARK(38);
    cnt_wait(C_HA, 7);
    MARK(40);
    poll(rsW, l128, fl_off(hbA, F_HA, 0), tag, 255);
    cnt_inc(C_RA);
    MARK(41);
    cnt_wait(C_HB, 7);
    MARK(42);
    poll(rsW, l128, fl_off(hbA, F_HB, 0), tag, 255);
    cnt_inc(C_RB);
    MARK(47);
    FINAL_SETUP();
    if (b == 0) {
#pragma clang loop unroll(disable)
      for (int k = 0; k < 4; ++k) poll(rsW, l128, WS_WFLAG + k * 8192, tag, ~0ull);
      set_flag(rsW, zero0, WS_EPOCH, tag, 1);
    }
  }
  {
    BAR();
    MARK(52);
    f32x4 s0, s1;
    if (mt < 2) {
      s0 = *(LAS f32x4*)p; s1 = *(LAS f32x4*)(p + 256);
#pragma unroll
      for (int v = 1; v < 7; ++v) { s0 += *(LAS f32x4*)(p + v * 12288); s1 += *(LAS f32x4*)(p + v * 12288 + 256); }
    } else {
      s0 = *(LAS f32x4*)p; s1 = *(LAS f32x4*)(p + 256);
#pragma unroll
      for (int v = 1; v < 7; ++v) { s0 += *(LAS f32x4*)(p + v * 8192); s1 += *(LAS f32x4*)(p + v * 8192 + 256); }
    }
    u32x4 o;
#pragma unroll
    for (int e = 0; e < 4; ++e) o[e] = pk2(fma2(gf[e], e < 2 ? f32x2{s0[2 * e], s0[2 * e + 1]} : f32x2{s1[2 * e - 4], s1[2 * e - 3]}, cf[e]));
    *op = o;
  }
  MARK(53);
  asm volatile(".Lpf_end:");
}
}

extern "C" void kernel_launch(void* const* d_in, const int* in_sizes, int n_in, void* d_out, int out_size, void* d_ws, size_t ws_size, hipStream_t stream) {
  fused_block<<<256, 512, 0, stream>>>((const u16*)d_in[0], (const u16*)d_in[9], (const u16*)d_in[8], (char*)d_ws, (const u16*)d_in[1], (const u16*)d_in[4],
                                       (const u16*)d_in[6], (const u16*)d_in[2], (const u16*)d_in[5], (const u16*)d_in[7], (u16*)d_out);
}
```
